```python
import torch

def _softmax(x, axis=-1):
    return torch.softmax(x, dim=axis)
def _gelu(x):
    return torch.nn.functional.gelu(x, approximate="tanh")
def _silu(x):
    return torch.nn.functional.silu(x)
def _sigmoid(x):
    return torch.sigmoid(x)
def _layernorm(x, axis=-1, eps=1e-5):
    m = x.mean(dim=axis, keepdim=True)
    v = x.var(dim=axis, keepdim=True, unbiased=False)
    return (x - m) / torch.sqrt(v + eps)
def _scatter_add(tgt, idx, src, axis=0):
    return tgt.clone().index_add_(axis, idx, src)
def _segment_sum(src, seg, n):
    out = torch.zeros((n,) + src.shape[1:], dtype=src.dtype)
    return out.index_add_(0, seg, src)
def _pad(x, pw):
    flat = tuple(v for pair in reversed(pw) for v in pair)
    return torch.nn.functional.pad(x, flat)

kernel_name = 'gated_mix__linear_attn_block__linear_kernel_hedgehog__prompt'

def setup_inputs():

    _w = (lambda a: (lambda t: t.to(torch.bfloat16) if t.is_floating_point() else t)(torch.as_tensor(a)))
    def _A_setup():
        B, S, D = 1, 2048, 576
        return {
            "x": _w((lambda *s: torch.randn(*s, dtype=torch.bfloat16) * 0.05)(B, S, D)),
            "wqkv": _w((lambda *s: torch.randn(*s, dtype=torch.bfloat16) * 1.6)(D, 3 * D)),
            "wo": _w((lambda *s: torch.randn(*s, dtype=torch.bfloat16) * 0.1)(D, D)),
        }
    _A_in = _A_setup()

    def _B_setup():
        B, S, D, H = 1, 2048, 576, 32
        Dh = D // H
        return {
            "x": _w((lambda *s: torch.randn(*s, dtype=torch.bfloat16) * 0.05)(B, S, D)),
            "wqkv": _w((lambda *s: torch.randn(*s, dtype=torch.bfloat16) * 1.6)(D, 3 * D)),
            "w_phi": _w((lambda *s: torch.randn(*s, dtype=torch.bfloat16) * 1.6)(H, Dh, Dh) * (Dh ** -0.5)),
            "wo": _w((lambda *s: torch.randn(*s, dtype=torch.bfloat16) * 0.05)(D, D)),
        }
    _B_in = _B_setup()

    return {
        "A_x": _A_in["x"],
        "A_wqkv": _A_in["wqkv"],
        "A_wo": _A_in["wo"],
        "B_x": _B_in["x"],
        "B_wqkv": _B_in["wqkv"],
        "B_w_phi": _B_in["w_phi"],
        "B_wo": _B_in["wo"],
        "gate_w": _w((lambda *s: torch.randn(*s, dtype=torch.bfloat16) * 0.05)(576) * 16.0),
    }

def reference(A_x, A_wqkv, A_wo, B_x, B_wqkv, B_w_phi, B_wo, gate_w):
    def _A_ref(x, wqkv, wo):
        B, S, D = x.shape
        H = 32; Dh = D // H
        qkv = (lambda x, *d: x.permute(*d))((x @ wqkv).reshape(B, S, 3, H, Dh), 2, 0, 3, 1, 4)
        q, k, v = qkv[0], qkv[1], qkv[2]
        phi = lambda t: torch.where(t > 0, t + 1, torch.exp(t)) + 1e-6
        pq, pk = phi(q), phi(k)
        kv = torch.einsum("bhsd,bhse->bhde", pk, v)
        z = torch.einsum("bhsd->bhd", pk)
        num = torch.einsum("bhqd,bhde->bhqe", pq, kv)
        den = torch.einsum("bhqd,bhd->bhq", pq, z)[..., None] + 1e-6
        o = num / den
        return (lambda x, *d: x.permute(*d))(o, 0, 2, 1, 3).reshape(B, S, D) @ wo

    def _B_ref(x, wqkv, w_phi, wo):
        B, S, D = x.shape
        H = 32; Dh = D // H
        qkv = (lambda x, *d: x.permute(*d))((x @ wqkv).reshape(B, S, 3, H, Dh), 2, 0, 3, 1, 4)
        q, k, v = qkv[0], qkv[1], qkv[2]
        pq = torch.einsum("bhsd,hdf->bhsf", q, w_phi)
        pk = torch.einsum("bhsd,hdf->bhsf", k, w_phi)
        phi_q = torch.concatenate([_softmax(pq, axis=-1), _softmax(-pq, axis=-1)], axis=-1)
        phi_k = torch.concatenate([_softmax(pk, axis=-1), _softmax(-pk, axis=-1)], axis=-1)
        kv = torch.einsum("bhsf,bhsd->bhfd", phi_k, v)
        z  = torch.einsum("bhsf->bhf", phi_k)
        num = torch.einsum("bhqf,bhfd->bhqd", phi_q, kv)
        den = torch.einsum("bhqf,bhf->bhq", phi_q, z)[..., None] + 1e-6
        o = num / den
        return (lambda x, *d: x.permute(*d))(o, 0, 2, 1, 3).reshape(B, S, D) @ wo

    x = A_x
    g = _sigmoid((x * gate_w).sum(-1, keepdims=True))
    a = _A_ref(x, A_wqkv, A_wo)
    b = _B_ref(x, B_wqkv, B_w_phi, B_wo)
    return g * a + (1 - g) * b
```

```cpp
#include <hip/hip_runtime.h>
#include <hip/hip_bf16.h>
#include <stdint.h>

using bf16 = __hip_bfloat16;
using bf16x8 = __attribute__((ext_vector_type(8))) short;
using f32x4 = __attribute__((ext_vector_type(4))) float;

static constexpr int S = 2048, D = 576, H = 32, DH = 18, N1 = 3 * D;

__device__ __forceinline__ float bfr(float v) { return __bfloat162float(__float2bfloat16(v)); }

__global__ __launch_bounds__(256) void gemm_simple(const bf16* __restrict__ A, int lda,
                                                   const bf16* __restrict__ B0, const bf16* __restrict__ B1,
                                                   int ldb, int ksplit,
                                                   bf16* __restrict__ C, int ldc, int K) {
  __shared__ __attribute__((aligned(16))) bf16 sA[64][40];
  __shared__ __attribute__((aligned(16))) bf16 sB[64][40];
  const int tid = threadIdx.x, lane = tid & 63, wid = tid >> 6;
  const int wr = wid >> 1, wc = wid & 1, fr = lane & 15, fq = lane >> 4;
  const int m0 = blockIdx.y * 64, n0 = blockIdx.x * 64;
  f32x4 acc[2][2] = {};
  for (int k0 = 0; k0 < K; k0 += 32) {
    {
      int r = tid >> 2, c = (tid & 3) * 8;
      *(uint4*)&sA[r][c] = *(const uint4*)&A[(size_t)(m0 + r) * lda + k0 + c];
    }
    {
      int kk = tid >> 3, c = (tid & 7) * 8;
      int kg = k0 + kk;
      const bf16* src = (kg < ksplit) ? (B0 + (size_t)kg * ldb) : (B1 + (size_t)(kg - ksplit) * ldb);
      uint4 v = *(const uint4*)&src[n0 + c];
      const bf16* e = (const bf16*)&v;
#pragma unroll
      for (int j = 0; j < 8; ++j) sB[c + j][kk] = e[j];
    }
    __syncthreads();
    bf16x8 a[2], b[2];
#pragma unroll
    for (int m = 0; m < 2; ++m) a[m] = *(const bf16x8*)&sA[wr * 32 + m * 16 + fr][fq * 8];
#pragma unroll
    for (int n = 0; n < 2; ++n) b[n] = *(const bf16x8*)&sB[wc * 32 + n * 16 + fr][fq * 8];
#pragma unroll
    for (int m = 0; m < 2; ++m)
#pragma unroll
      for (int n = 0; n < 2; ++n) acc[m][n] = __builtin_amdgcn_mfma_f32_16x16x32_bf16(a[m], b[n], acc[m][n], 0, 0, 0);
    __syncthreads();
  }
#pragma unroll
  for (int m = 0; m < 2; ++m)
#pragma unroll
    for (int n = 0; n < 2; ++n)
#pragma unroll
      for (int j = 0; j < 4; ++j)
        C[(size_t)(m0 + wr * 32 + m * 16 + fq * 4 + j) * ldc + n0 + wc * 32 + n * 16 + fr] = __float2bfloat16(acc[m][n][j]);
}

__global__ __launch_bounds__(256) void kv_kernel(const bf16* __restrict__ qkv,
                                                 const bf16* __restrict__ wphi, float* __restrict__ kvz) {
  const int br = blockIdx.x >> 5, h = blockIdx.x & 31;
  const int F = br ? 36 : 18;
  __shared__ float sphi[64][37];
  __shared__ float sv[64][20];
  __shared__ float swp[18][18];
  const int tid = threadIdx.x;
  for (int i = tid; i < 324; i += 256) swp[i / 18][i % 18] = __bfloat162float(wphi[h * 324 + i]);
  float acc[3] = {0.f, 0.f, 0.f};
  __syncthreads();
  for (int r0 = 0; r0 < S; r0 += 64) {
    if (tid < 64) {
      const int r = r0 + tid;
      const bf16* row = qkv + (size_t)r * (2 * N1) + br * N1;
      float k[18];
      for (int d = 0; d < 18; ++d) k[d] = __bfloat162float(row[D + h * 18 + d]);
      for (int d = 0; d < 18; ++d) sv[tid][d] = __bfloat162float(row[2 * D + h * 18 + d]);
      sv[tid][18] = 1.f;
      if (br == 0) {
        for (int d = 0; d < 18; ++d) {
          float t = k[d];
          float p = t > 0.f ? bfr(t + 1.f) : bfr(__expf(t));
          sphi[tid][d] = bfr(p + 1e-6f);
        }
      } else {
        float p[18], mx = -1e30f, mn = 1e30f;
        for (int f = 0; f < 18; ++f) {
          float s = 0.f;
          for (int d = 0; d < 18; ++d) s += k[d] * swp[d][f];
          p[f] = bfr(s);
          mx = fmaxf(mx, p[f]); mn = fminf(mn, p[f]);
        }
        float e1[18], e2[18], s1 = 0.f, s2 = 0.f;
        for (int f = 0; f < 18; ++f) { e1[f] = __expf(p[f] - mx); s1 += e1[f]; e2[f] = __expf(mn - p[f]); s2 += e2[f]; }
        for (int f = 0; f < 18; ++f) { sphi[tid][f] = bfr(e1[f] / s1); sphi[tid][18 + f] = bfr(e2[f] / s2); }
      }
    }
    __syncthreads();
#pragma unroll
    for (int i = 0; i < 3; ++i) {
      int idx = tid + i * 256;
      if (idx < F * 19) {
        int f = idx / 19, e = idx % 19;
        float a = acc[i];
        for (int rr = 0; rr < 64; ++rr) a += sphi[rr][f] * sv[rr][e];
        acc[i] = a;
      }
    }
    __syncthreads();
  }
#pragma unroll
  for (int i = 0; i < 3; ++i) {
    int idx = tid + i * 256;
    if (idx < F * 19) kvz[(size_t)(br * 32 + h) * (36 * 19) + idx] = bfr(acc[i]);
  }
}

__global__ __launch_bounds__(256) void o_kernel(const bf16* __restrict__ qkv, const bf16* __restrict__ wphi,
                                                const float* __restrict__ kvz, const bf16* __restrict__ x,
                                                const bf16* __restrict__ gate_w, bf16* __restrict__ O) {
  const int r = blockIdx.x;
  const int tid = threadIdx.x;
  __shared__ float red[256];
  float s = 0.f;
  for (int d = tid; d < D; d += 256) s += bfr(__bfloat162float(x[(size_t)r * D + d]) * __bfloat162float(gate_w[d]));
  red[tid] = s;
  __syncthreads();
  for (int o = 128; o > 0; o >>= 1) { if (tid < o) red[tid] += red[tid + o]; __syncthreads(); }
  const float gsum = bfr(red[0]);
  const float g = bfr(1.f / (1.f + __expf(-gsum)));
  if (tid >= 64) return;
  const int br = tid >> 5, h = tid & 31;
  const bf16* row = qkv + (size_t)r * (2 * N1) + br * N1;
  float q[18];
#pragma unroll
  for (int d = 0; d < 18; ++d) q[d] = __bfloat162float(row[h * 18 + d]);
  float phi[36];
  if (br == 0) {
#pragma unroll
    for (int d = 0; d < 18; ++d) {
      float t = q[d];
      float p = t > 0.f ? bfr(t + 1.f) : bfr(__expf(t));
      phi[d] = bfr(p + 1e-6f);
    }
  } else {
    float p[18], mx = -1e30f, mn = 1e30f;
#pragma unroll
    for (int f = 0; f < 18; ++f) {
      float sm = 0.f;
#pragma unroll
      for (int d = 0; d < 18; ++d) sm += q[d] * __bfloat162float(wphi[h * 324 + d * 18 + f]);
      p[f] = bfr(sm);
      mx = fmaxf(mx, p[f]); mn = fminf(mn, p[f]);
    }
    float s1 = 0.f, s2 = 0.f;
#pragma unroll
    for (int f = 0; f < 18; ++f) { phi[f] = __expf(p[f] - mx); s1 += phi[f]; phi[18 + f] = __expf(mn - p[f]); s2 += phi[18 + f]; }
#pragma unroll
    for (int f = 0; f < 18; ++f) { phi[f] = bfr(phi[f] / s1); phi[18 + f] = bfr(phi[18 + f] / s2); }
  }
  const float* kv = kvz + (size_t)(br * 32 + h) * (36 * 19);
  const float sc = br ? (1.f - g) : g;
  if (br == 0) {
    float den = 0.f;
#pragma unroll
    for (int f = 0; f < 18; ++f) den += phi[f] * kv[f * 19 + 18];
    den = bfr(bfr(den) + 1e-6f);
    for (int e = 0; e < 18; ++e) {
      float num = 0.f;
#pragma unroll
      for (int f = 0; f < 18; ++f) num += phi[f] * kv[f * 19 + e];
      float o = bfr(bfr(num) / den);
      O[(size_t)r * (2 * D) + br * D + h * 18 + e] = __float2bfloat16(o * sc);
    }
  } else {
    float den = 0.f;
#pragma unroll
    for (int f = 0; f < 36; ++f) den += phi[f] * kv[f * 19 + 18];
    den = bfr(bfr(den) + 1e-6f);
    for (int e = 0; e < 18; ++e) {
      float num = 0.f;
#pragma unroll
      for (int f = 0; f < 36; ++f) num += phi[f] * kv[f * 19 + e];
      float o = bfr(bfr(num) / den);
      O[(size_t)r * (2 * D) + br * D + h * 18 + e] = __float2bfloat16(o * sc);
    }
  }
}

extern "C" void kernel_launch(void* const* d_in, const int* in_sizes, int n_in, void* d_out, int out_size,
                              void* d_ws, size_t ws_size, hipStream_t stream) {
  const bf16* x = (const bf16*)d_in[0];
  const bf16* wa = (const bf16*)d_in[1];
  const bf16* woa = (const bf16*)d_in[2];
  const bf16* wb = (const bf16*)d_in[4];
  const bf16* wphi = (const bf16*)d_in[5];
  const bf16* wob = (const bf16*)d_in[6];
  const bf16* gw = (const bf16*)d_in[7];
  bf16* out = (bf16*)d_out;
  char* ws = (char*)d_ws;
  bf16* qkv = (bf16*)ws;
  float* kvz = (float*)(ws + (size_t)S * 2 * N1 * 2);
  bf16* O = (bf16*)(ws + (size_t)S * 2 * N1 * 2 + 65536 * 4);
  gemm_simple<<<dim3(N1 / 64, S / 64), 256, 0, stream>>>(x, D, wa, wa, N1, 1 << 30, qkv, 2 * N1, D);
  gemm_simple<<<dim3(N1 / 64, S / 64), 256, 0, stream>>>(x, D, wb, wb, N1, 1 << 30, qkv + N1, 2 * N1, D);
  kv_kernel<<<64, 256, 0, stream>>>(qkv, wphi, kvz);
  o_kernel<<<S, 256, 0, stream>>>(qkv, wphi, kvz, x, gw, O);
  gemm_simple<<<dim3(D / 64, S / 64), 256, 0, stream>>>(O, 2 * D, woa, wob, D, D, out, D, 2 * D);
}
```

```cpp
#include <hip/hip_runtime.h>
#include <hip/hip_bf16.h>
#include <stdint.h>

using bf16 = __hip_bfloat16;
typedef __attribute__((ext_vector_type(8))) short bf16x8;
typedef __attribute__((ext_vector_type(4))) short s16x4;
typedef __attribute__((ext_vector_type(4))) float f32x4;
typedef __attribute__((ext_vector_type(4))) unsigned u32x4;
typedef __attribute__((ext_vector_type(2))) unsigned u32x2;
#define LDS_AS __attribute__((address_space(3)))

static constexpr int S_ = 2048, D_ = 576, N1_ = 1728;

__device__ __forceinline__ unsigned short f2bf(float v) { return __bfloat16_as_ushort(__float2bfloat16(v)); }
__device__ __forceinline__ float bf2f(unsigned short b) { return __uint_as_float(((unsigned)b) << 16); }
__device__ __forceinline__ float bfr(float v) { return bf2f(f2bf(v)); }
__device__ __forceinline__ unsigned pk2(float lo, float hi) { return (unsigned)f2bf(lo) | ((unsigned)f2bf(hi) << 16); }
__device__ __forceinline__ void glds16(const void* g, void* l) {
  __builtin_amdgcn_global_load_lds((const unsigned*)g, (unsigned*)l, 16, 0, 0);
}
__device__ __forceinline__ s16x4 tr_read(const char* p) {
  return __builtin_amdgcn_ds_read_tr16_b64_v4i16((LDS_AS s16x4*)p);
}
__device__ __forceinline__ bf16x8 cat8(s16x4 a, s16x4 b) {
  bf16x8 r; r[0] = a[0]; r[1] = a[1]; r[2] = a[2]; r[3] = a[3]; r[4] = b[0]; r[5] = b[1]; r[6] = b[2]; r[7] = b[3]; return r;
}
#define MFMA16(a, b, c) __builtin_amdgcn_mfma_f32_16x16x32_bf16((a), (b), (c), 0, 0, 0)
#define WAIT_VM(n) asm volatile("s_waitcnt vmcnt(" #n ")" ::: "memory")
#define RAW_BAR() do { asm volatile("s_waitcnt lgkmcnt(0)" ::: "memory"); __builtin_amdgcn_s_barrier(); asm volatile("" ::: "memory"); } while (0)

__device__ __forceinline__ float phiA(float t) {
  float p = t > 0.f ? bfr(t + 1.f) : bfr(__expf(t));
  return bfr(p + 1e-6f);
}

namespace k1 {
constexpr int XS = 16384, WS = 28672, STAGE = XS + WS;
constexpr int RS = 296;
constexpr int PHS = 200;
constexpr int PH_OFF = 128 * RS * 2;
constexpr int LDS_BYTES = 3 * STAGE;
}
static constexpr int KVH = 960;

__global__ __launch_bounds__(512, 2) void k1_qkv(const bf16* __restrict__ x, const bf16* __restrict__ wA,
                                                 const bf16* __restrict__ wB, const bf16* __restrict__ wphi,
                                                 const bf16* __restrict__ gate_w, bf16* __restrict__ Q,
                                                 float* __restrict__ KVP, float* __restrict__ G) {
  using namespace k1;
  __shared__ __attribute__((aligned(16))) char smem[LDS_BYTES];
  const int tid = threadIdx.x, lane = tid & 63;
  const int wid = __builtin_amdgcn_readfirstlane(tid >> 6);
  const int bid = blockIdx.x, xcd = bid & 7, idx = bid >> 3;
  const int cg = (xcd & 3) * 4 + (idx & 3), rb = (xcd >> 2) * 8 + (idx >> 2);
  const int br = cg >> 3, hg = cg & 7;
  const bf16* W = br ? wB : wA;
  const int fr = lane & 15, fg = lane >> 4;

  const int grow = rb * 128 + cg * 8 + wid;
  u32x4 gx0 = {}, gx1 = {}, gw0 = {}, gw1 = {};
  if (lane < 36) {
    const u32x4* px = (const u32x4*)(x + (size_t)grow * D_ + lane * 16);
    const u32x4* pw = (const u32x4*)(gate_w + lane * 16);
    gx0 = px[0]; gx1 = px[1]; gw0 = pw[0]; gw1 = pw[1];
  }
  const int ph = wid & 3;
  bf16x8 wf[2];
  {
    const unsigned short* wp = (const unsigned short*)wphi + (size_t)(hg * 4 + ph) * 324;
#pragma unroll
    for (int t = 0; t < 2; ++t) {
      const int f = fr + 16 * t;
#pragma unroll
      for (int j = 0; j < 8; ++j) {
        const int d = 8 * fg + j;
        unsigned short v = 0;
        if (br && d < 18 && f < 18) v = wp[d * 18 + f];
        wf[t][j] = (short)v;
      }
    }
  }

  int xoff[2];
#pragma unroll
  for (int i = 0; i < 2; ++i) {
    const int row = 16 * wid + 8 * i + (lane >> 3);
    const int c = (lane & 7) ^ ((row >> 1) & 7);
    xoff[i] = (rb * 128 + row) * D_ + c * 8;
  }
  int woff[4];
#pragma unroll
  for (int m = 0; m < 4; ++m) {
    const int j = wid + 8 * m;
    const int st = 4 * j + fg, octet = st / 14, cb = st % 14;
    const int rowslot = fr >> 1, half = fr & 1;
    const int kio = rowslot ^ ((octet & 1) << 2);
    int gc = cb * 16 + half * 8;
    if (gc >= 216) gc = 208;
    const int seg = gc / 72, within = gc - 72 * seg;
    woff[m] = (octet * 8 + kio) * N1_ + seg * D_ + hg * 72 + within;
  }
  auto issue = [&](int t) {
    char* sb = smem + (t % 3) * STAGE;
#pragma unroll
    for (int i = 0; i < 2; ++i) glds16(x + xoff[i] + t * 64, sb + (16 * wid + 8 * i) * 128);
#pragma unroll
    for (int m = 0; m < 4; ++m) {
      const int j = wid + 8 * m;
      if (j < 28) glds16(W + woff[m] + (size_t)t * 64 * N1_, sb + XS + j * 1024);
    }
  };

  const int mi = wid & 1, nj = wid >> 1;
  const int ct0 = nj < 2 ? nj * 4 : 8 + (nj - 2) * 3;
  const bool four = nj < 2;
  const int q4 = fr >> 2, p4 = fr & 3;
  f32x4 acc[4][4];
#pragma unroll
  for (int m = 0; m < 4; ++m)
#pragma unroll
    for (int n = 0; n < 4; ++n) acc[m][n] = f32x4{0.f, 0.f, 0.f, 0.f};

  issue(0);
  issue(1);
#pragma unroll
  for (int t = 0; t < 9; ++t) {
    if (t < 8) { if (wid < 4) WAIT_VM(6); else WAIT_VM(5); }
    else WAIT_VM(0);
    RAW_BAR();
    if (t + 2 < 9) issue(t + 2);
    const char* sb = smem + (t % 3) * STAGE;
#pragma unroll
    for (int ks = 0; ks < 2; ++ks) {
      bf16x8 a[4], b[4];
#pragma unroll
      for (int m = 0; m < 4; ++m)
        a[m] = *(const bf16x8*)(sb + (64 * mi + 16 * m + fr) * 128 + (((4 * ks + fg) ^ (fr >> 1)) * 16));
      const char* bbase = sb + XS + ((4 * ks + fg) * 14 + ct0) * 256 + p4 * 8;
      const int rs0 = ((0 + q4) ^ ((fg & 1) << 2)) * 32, rs1 = ((4 + q4) ^ ((fg & 1) << 2)) * 32;
#pragma unroll
      for (int n = 0; n < 3; ++n) b[n] = cat8(tr_read(bbase + n * 256 + rs0), tr_read(bbase + n * 256 + rs1));
      if (four) b[3] = cat8(tr_read(bbase + 3 * 256 + rs0), tr_read(bbase + 3 * 256 + rs1));
#pragma unroll
      for (int m = 0; m < 4; ++m)
#pragma unroll
        for (int n = 0; n < 3; ++n) acc[m][n] = MFMA16(a[m], b[n], acc[m][n]);
      if (four) {
#pragma unroll
        for (int m = 0; m < 4; ++m) acc[m][3] = MFMA16(a[m], b[3], acc[m][3]);
      }
    }
  }

  unsigned short* CT = (unsigned short*)smem;
#pragma unroll
  for (int n = 0; n < 4; ++n) {
    if (n == 3 && !four) continue;
    const int gc = (ct0 + n) * 16 + fr;
    if (gc < 216) {
      const int seg = gc / 72, rem = gc - 72 * seg, h = rem / 18, d = rem - 18 * h;
      const int slot = (seg * 4 + h) * 24 + d;
      const bool dophi = (br == 0) && (seg == 1);
#pragma unroll
      for (int m = 0; m < 4; ++m)
#pragma unroll
        for (int i = 0; i < 4; ++i) {
          const int row = 64 * mi + 16 * m + 4 * fg + i;
          unsigned short v = f2bf(acc[m][n][i]);
          if (dophi) v = f2bf(phiA(bf2f(v)));
          CT[row * RS + slot] = v;
        }
    }
  }
  {
    const int row = tid >> 2, h = tid & 3;
    *(unsigned*)&CT[row * RS + (8 + h) * 24 + 18] = 0x00003F80u;
  }
  __syncthreads();

  {
    unsigned* Qd = (unsigned*)Q;
#pragma unroll
    for (int it = 0; it < 9; ++it) {
      const int w = tid + it * 512;
      const int hh = w / 1152, rem = w - hh * 1152, row = rem / 9, dw = rem - row * 9;
      const unsigned v = *(const unsigned*)&CT[row * RS + hh * 24 + 2 * dw];
      Qd[((size_t)(br * 32 + hg * 4 + hh) * S_ + rb * 128 + row) * 9 + dw] = v;
    }
  }
  {
    float s = 0.f;
    const unsigned* a0 = (const unsigned*)&gx0; const unsigned* a1 = (const unsigned*)&gx1;
    const unsigned* b0 = (const unsigned*)&gw0; const unsigned* b1 = (const unsigned*)&gw1;
#pragma unroll
    for (int j = 0; j < 4; ++j) {
      s += bfr(bf2f(a0[j] & 0xffff) * bf2f(b0[j] & 0xffff)) + bfr(bf2f(a0[j] >> 16) * bf2f(b0[j] >> 16));
      s += bfr(bf2f(a1[j] & 0xffff) * bf2f(b1[j] & 0xffff)) + bfr(bf2f(a1[j] >> 16) * bf2f(b1[j] >> 16));
    }
#pragma unroll
    for (int o = 32; o > 0; o >>= 1) s += __shfl_xor(s, o);
    if (lane == 0) G[grow] = 1.f / (1.f + __expf(-bfr(s)));
  }
  unsigned short* PH = (unsigned short*)(smem + PH_OFF);
  if (br) {
#pragma unroll
    for (int j = 0; j < 4; ++j) {
      const int row = ((wid >> 2) * 4 + j) * 16 + fr;
      u32x4 kb = *(const u32x4*)&CT[row * RS + (4 + ph) * 24 + 8 * fg];
      if (fg == 2) { kb[1] = 0u; kb[2] = 0u; kb[3] = 0u; }
      if (fg == 3) { kb = u32x4{0u, 0u, 0u, 0u}; }
      const bf16x8 kf = __builtin_bit_cast(bf16x8, kb);
      f32x4 p0 = MFMA16(wf[0], kf, (f32x4{0.f, 0.f, 0.f, 0.f}));
      f32x4 p1 = MFMA16(wf[1], kf, (f32x4{0.f, 0.f, 0.f, 0.f}));
      float pv[6];
#pragma unroll
      for (int i = 0; i < 4; ++i) pv[i] = bfr(p0[i]);
      pv[4] = bfr(p1[0]); pv[5] = bfr(p1[1]);
      const bool ex = (fg == 0);
      float mx = fmaxf(fmaxf(pv[0], pv[1]), fmaxf(pv[2], pv[3]));
      float mn = fminf(fminf(pv[0], pv[1]), fminf(pv[2], pv[3]));
      if (ex) { mx = fmaxf(mx, fmaxf(pv[4], pv[5])); mn = fminf(mn, fminf(pv[4], pv[5])); }
      mx = fmaxf(mx, __shfl_xor(mx, 16)); mx = fmaxf(mx, __shfl_xor(mx, 32));
      mn = fminf(mn, __shfl_xor(mn, 16)); mn = fminf(mn, __shfl_xor(mn, 32));
      float ep[6], en[6], sp = 0.f, sn = 0.f;
#pragma unroll
      for (int i = 0; i < 6; ++i) {
        const bool valid = (i < 4) || ex;
        ep[i] = valid ? __expf(pv[i] - mx) : 0.f;
        en[i] = valid ? __expf(mn - pv[i]) : 0.f;
        sp += ep[i]; sn += en[i];
      }
      sp += __shfl_xor(sp, 16); sp += __shfl_xor(sp, 32);
      sn += __shfl_xor(sn, 16); sn += __shfl_xor(sn, 32);
      const float ip = 1.f / sp, in_ = 1.f / sn;
      unsigned short* dst = PH + row * PHS + ph * 48;
      *(u32x2*)&dst[4 * fg] = u32x2{pk2(ep[0] * ip, ep[1] * ip), pk2(ep[2] * ip, ep[3] * ip)};
      *(u32x2*)&dst[20 + 4 * fg] = u32x2{pk2(en[0] * in_, en[1] * in_), pk2(en[2] * in_, en[3] * in_)};
      if (ex) {
        *(unsigned*)&dst[16] = pk2(ep[4] * ip, ep[5] * ip);
        *(unsigned*)&dst[36] = pk2(en[4] * in_, en[5] * in_);
      }
    }
  }
  __syncthreads();

  {
    const int half = wid >> 2;
    const char* abase; int ars;
    if (br) { abase = (const char*)(PH + ph * 48); ars = PHS * 2; }
    else    { abase = (const char*)(CT + (4 + ph) * 24); ars = RS * 2; }
    const char* bbase = (const char*)(CT + (8 + ph) * 24);
    float* dst = KVP + ((size_t)(cg * 16 + rb) * 4 + ph) * KVH;
    const int ncomb = br ? 3 : 2;
#pragma unroll
    for (int c = 0; c < 3; ++c) {
      if (c >= ncomb) break;
      int mt, nt;
      if (br) { const int id = half * 3 + c; mt = id >> 1; nt = id & 1; }
      else    { mt = half; nt = c; }
      f32x4 a3 = {0.f, 0.f, 0.f, 0.f};
#pragma unroll
      for (int ks = 0; ks < 4; ++ks) {
        const int r0 = 32 * ks + 8 * fg + q4;
        const bf16x8 af = cat8(tr_read(abase + r0 * ars + (16 * mt + 4 * p4) * 2),
                               tr_read(abase + (r0 + 4) * ars + (16 * mt + 4 * p4) * 2));
        const bf16x8 bfv = cat8(tr_read(bbase + r0 * (RS * 2) + (16 * nt + 4 * p4) * 2),
                                tr_read(bbase + (r0 + 4) * (RS * 2) + (16 * nt + 4 * p4) * 2));
        a3 = MFMA16(af, bfv, a3);
      }
      const int e = 16 * nt + fr;
      if (e < 19) {
#pragma unroll
        for (int i = 0; i < 4; ++i) {
          const int slot = 16 * mt + 4 * fg + i;
          const bool ok = br ? (slot < 18 || (slot >= 20 && slot < 38)) : (slot < 18);
          if (ok) dst[slot * 20 + e] = a3[i];
        }
      }
    }
  }
}

namespace k2 {
constexpr int KTA_S = 40, KTB_S = 72;
constexpr int KTA_BYTES = 32 * KTA_S * 2;
constexpr int KTB_BYTES = 32 * KTB_S * 2;
}

__global__ __launch_bounds__(512, 2) void k2_attn(const bf16* __restrict__ Q, const float* __restrict__ KVP,
                                                  const float* __restrict__ G, const bf16* __restrict__ wphi,
                                                  bf16* __restrict__ O) {
  using namespace k2;
  __shared__ __attribute__((aligned(16))) char smem[KTA_BYTES + KTB_BYTES];
  unsigned short* KTA = (unsigned short*)smem;
  unsigned short* KTB = (unsigned short*)(smem + KTA_BYTES);
  const int tid = threadIdx.x, lane = tid & 63;
  const int wid = __builtin_amdgcn_readfirstlane(tid >> 6);
  const int fr = lane & 15, fg = lane >> 4;
  const int h = blockIdx.x >> 3, chunk = blockIdx.x & 7;

  u32x4 qf[2][2];
  float gt[2];
#pragma unroll
  for (int j = 0; j < 2; ++j) {
    const int row = chunk * 256 + wid * 32 + j * 16 + fr;
    gt[j] = G[row];
#pragma unroll
    for (int b = 0; b < 2; ++b) {
      const unsigned* src = (const unsigned*)Q + ((size_t)(b * 32 + h) * S_ + row) * 9 + 4 * fg;
      u32x4 v = {0u, 0u, 0u, 0u};
      if (fg < 2) { v[0] = src[0]; v[1] = src[1]; v[2] = src[2]; v[3] = src[3]; }
      else if (fg == 2) { v[0] = src[0]; }
      qf[j][b] = v;
    }
  }
  bf16x8 wf[2];
  {
    const unsigned short* wp = (const unsigned short*)wphi + (size_t)h * 324;
#pragma unroll
    for (int t = 0; t < 2; ++t) {
      const int f = fr + 16 * t;
#pragma unroll
      for (int j = 0; j < 8; ++j) {
        const int d = 8 * fg + j;
        unsigned short v = 0;
        if (d < 18 && f < 18) v = wp[d * 18 + f];
        wf[t][j] = (short)v;
      }
    }
  }
  for (int i = tid; i < (KTA_BYTES + KTB_BYTES) / 4; i += 512) ((unsigned*)smem)[i] = 0u;
  float4 sum = {0.f, 0.f, 0.f, 0.f};
  int frow = 0, e4 = 0, bb = 0, ff = 0;
  if (tid < 270) {
    frow = tid / 5; e4 = tid - frow * 5; bb = frow >= 18; ff = frow - 18 * bb;
    const int slot = (bb && ff >= 18) ? ff + 2 : ff;
    const int cg = bb * 8 + (h >> 2), hh = h & 3;
    const float* src = KVP + ((size_t)(cg * 16) * 4 + hh) * KVH + slot * 20 + e4 * 4;
#pragma unroll
    for (int rb = 0; rb < 16; ++rb) {
      const float4 v = *(const float4*)(src + (size_t)rb * 4 * KVH);
      sum.x += v.x; sum.y += v.y; sum.z += v.z; sum.w += v.w;
    }
  }
  __syncthreads();
  if (tid < 270) {
    const float sv[4] = {sum.x, sum.y, sum.z, sum.w};
    int kslot;
    if (!bb) kslot = ff;
    else { const int sg = ff / 18, f2 = ff - 18 * sg; kslot = f2 < 16 ? ((f2 >> 2) * 8 + sg * 4 + (f2 & 3)) : (32 + sg * 2 + (f2 - 16)); }
    unsigned short* KT = bb ? KTB : KTA;
    const int ss = bb ? KTB_S : KTA_S;
#pragma unroll
    for (int i = 0; i < 4; ++i) {
      const int e = e4 * 4 + i;
      const unsigned short v = f2bf(sv[i]);
      if (e < 18) KT[e * ss + kslot] = v;
      else if (e == 18) { KT[19 * ss + kslot] = v; KT[23 * ss + kslot] = v; KT[27 * ss + kslot] = v; KT[31 * ss + kslot] = v; }
    }
  }
  __syncthreads();

  bf16x8 ka[2], kb0[2], kb1[2];
#pragma unroll
  for (int mt = 0; mt < 2; ++mt) {
    ka[mt] = *(const bf16x8*)&KTA[(16 * mt + fr) * KTA_S + 8 * fg];
    kb0[mt] = *(const bf16x8*)&KTB[(16 * mt + fr) * KTB_S + 8 * fg];
    kb1[mt] = *(const bf16x8*)&KTB[(16 * mt + fr) * KTB_S + 32 + 8 * fg];
  }
#pragma unroll
  for (int j = 0; j < 2; ++j) {
    const int row = chunk * 256 + wid * 32 + j * 16 + fr;
    unsigned* orow = (unsigned*)(O + (size_t)row * (2 * D_) + h * 18);
    {
      u32x4 qv = qf[j][0];
      if (fg == 2) { qv[1] = 0u; qv[2] = 0u; qv[3] = 0u; }
      unsigned pw[4];
#pragma unroll
      for (int w2 = 0; w2 < 4; ++w2) {
        const bool valid = (fg < 2) || (fg == 2 && w2 == 0);
        const float lo = phiA(bf2f(qv[w2] & 0xffff)), hi = phiA(bf2f(qv[w2] >> 16));
        pw[w2] = valid ? pk2(lo, hi) : 0u;
      }
      const bf16x8 pf = __builtin_bit_cast(bf16x8, u32x4{pw[0], pw[1], pw[2], pw[3]});
      const f32x4 t0 = MFMA16(ka[0], pf, (f32x4{0.f, 0.f, 0.f, 0.f}));
      const f32x4 t1 = MFMA16(ka[1], pf, (f32x4{0.f, 0.f, 0.f, 0.f}));
      const float den = bfr(bfr(t1[3]) + 1e-6f);
      const float sc = gt[j] / den;
      orow[2 * fg] = pk2(bfr(t0[0]) * sc, bfr(t0[1]) * sc);
      orow[2 * fg + 1] = pk2(bfr(t0[2]) * sc, bfr(t0[3]) * sc);
      if (fg == 0) orow[8] = pk2(bfr(t1[0]) * sc, bfr(t1[1]) * sc);
    }
    {
      const bf16x8 qb = __builtin_bit_cast(bf16x8, qf[j][1]);
      const f32x4 p0 = MFMA16(wf[0], qb, (f32x4{0.f, 0.f, 0.f, 0.f}));
      const f32x4 p1 = MFMA16(wf[1], qb, (f32x4{0.f, 0.f, 0.f, 0.f}));
      float pv[6];
#pragma unroll
      for (int i = 0; i < 4; ++i) pv[i] = bfr(p0[i]);
      pv[4] = bfr(p1[0]); pv[5] = bfr(p1[1]);
      const bool ex = (fg == 0);
      float mx = fmaxf(fmaxf(pv[0], pv[1]), fmaxf(pv[2], pv[3]));
      float mn = fminf(fminf(pv[0], pv[1]), fminf(pv[2], pv[3]));
      if (ex) { mx = fmaxf(mx, fmaxf(pv[4], pv[5])); mn = fminf(mn, fminf(pv[4], pv[5])); }
      mx = fmaxf(mx, __shfl_xor(mx, 16)); mx = fmaxf(mx, __shfl_xor(mx, 32));
      mn = fminf(mn, __shfl_xor(mn, 16)); mn = fminf(mn, __shfl_xor(mn, 32));
      float ep[6], en[6], sp = 0.f, sn = 0.f;
#pragma unroll
      for (int i = 0; i < 6; ++i) {
        const bool valid = (i < 4) || ex;
        ep[i] = valid ? __expf(pv[i] - mx) : 0.f;
        en[i] = valid ? __expf(mn - pv[i]) : 0.f;
        sp += ep[i]; sn += en[i];
      }
      sp += __shfl_xor(sp, 16); sp += __shfl_xor(sp, 32);
      sn += __shfl_xor(sn, 16); sn += __shfl_xor(sn, 32);
      const float ip = 1.f / sp, in_ = 1.f / sn;
      const bf16x8 ph0 = __builtin_bit_cast(bf16x8, u32x4{pk2(ep[0] * ip, ep[1] * ip), pk2(ep[2] * ip, ep[3] * ip),
                                                          pk2(en[0] * in_, en[1] * in_), pk2(en[2] * in_, en[3] * in_)});
      const bf16x8 ph1 = __builtin_bit_cast(bf16x8, u32x4{ex ? pk2(ep[4] * ip, ep[5] * ip) : 0u,
                                                          ex ? pk2(en[4] * in_, en[5] * in_) : 0u, 0u, 0u});
      f32x4 t0 = MFMA16(kb0[0], ph0, (f32x4{0.f, 0.f, 0.f, 0.f}));
      t0 = MFMA16(kb1[0], ph1, t0);
      f32x4 t1 = MFMA16(kb0[1], ph0, (f32x4{0.f, 0.f, 0.f, 0.f}));
      t1 = MFMA16(kb1[1], ph1, t1);
      const float den = bfr(bfr(t1[3]) + 1e-6f);
      const float sc = (1.f - gt[j]) / den;
      unsigned* ob = orow + D_ / 2;
      ob[2 * fg] = pk2(bfr(t0[0]) * sc, bfr(t0[1]) * sc);
      ob[2 * fg + 1] = pk2(bfr(t0[2]) * sc, bfr(t0[3]) * sc);
      if (fg == 0) ob[8] = pk2(bfr(t1[0]) * sc, bfr(t1[1]) * sc);
    }
  }
}

namespace k3 {
constexpr int AS = 16384, BS = 20480, STAGE = AS + BS;
constexpr int NS = 4, KT = 9;
constexpr int LDS_BYTES = NS * STAGE;
}

__global__ __launch_bounds__(512, 2) void k3_out(const bf16* __restrict__ O, const bf16* __restrict__ woA,
                                                 const bf16* __restrict__ woB, bf16* __restrict__ out) {
  using namespace k3;
  __shared__ __attribute__((aligned(16))) char smem[LDS_BYTES];
  const int tid = threadIdx.x, lane = tid & 63;
  const int wid = __builtin_amdgcn_readfirstlane(tid >> 6);
  const int bid = blockIdx.x, xcd = bid & 7, idx = bid >> 3;
  const int rb = xcd * 4 + (idx & 3), cb = idx >> 2;
  const int fr = lane & 15, fg = lane >> 4, q4 = fr >> 2, p4 = fr & 3;

  int aoff[2];
#pragma unroll
  for (int i = 0; i < 2; ++i) {
    const int row = 4 * (2 * wid + i) + fg;
    const int c = fr ^ (row & 15);
    aoff[i] = (rb * 64 + row) * (2 * D_) + c * 8;
  }
  int boff[3];
#pragma unroll
  for (int m = 0; m < 3; ++m) {
    const int j = wid + 8 * m;
    const int st = 4 * j + fg, octet = st / 5, cbk = st % 5;
    const int kio = (fr >> 1) ^ ((octet & 1) << 2);
    int gc = cbk * 16 + (fr & 1) * 8;
    if (gc >= 72) gc = 64;
    boff[m] = (octet * 8 + kio) * D_ + cb * 72 + gc;
  }
  auto issue = [&](int t) {
    char* sb = smem + (t % NS) * STAGE;
#pragma unroll
    for (int i = 0; i < 2; ++i) glds16(O + aoff[i] + t * 128, sb + (2 * wid + i) * 1024);
#pragma unroll
    for (int m = 0; m < 3; ++m) {
      const int j = wid + 8 * m;
      if (j < 20) {
        const int r = boff[m] / D_;
        const int kg = t * 128 + r;
        const bf16* src = (kg < D_) ? (woA + (size_t)t * 128 * D_ + boff[m]) : (woB + ((size_t)kg - D_ - r) * D_ + boff[m]);
        glds16(src, sb + AS + j * 1024);
      }
    }
  };

  const int mi = wid & 1, kq = wid >> 1;
  f32x4 acc[2][5];
#pragma unroll
  for (int m = 0; m < 2; ++m)
#pragma unroll
    for (int n = 0; n < 5; ++n) acc[m][n] = f32x4{0.f, 0.f, 0.f, 0.f};

  issue(0); issue(1); issue(2);
#pragma unroll
  for (int t = 0; t < KT; ++t) {
    if (t < KT - 2) { if (wid < 4) WAIT_VM(10); else WAIT_VM(8); }
    else if (t == KT - 2) { if (wid < 4) WAIT_VM(5); else WAIT_VM(4); }
    else WAIT_VM(0);
    RAW_BAR();
    if (t + 3 < KT) issue(t + 3);
    const char* sb = smem + (t % NS) * STAGE;
    {
      const int ks = kq;
      bf16x8 a[2], b[5];
#pragma unroll
      for (int m = 0; m < 2; ++m) {
        const int row = 32 * mi + 16 * m + fr;
        a[m] = *(const bf16x8*)(sb + row * 256 + (((4 * ks + fg) ^ (row & 15)) * 16));
      }
      const char* bbase = sb + AS + ((4 * ks + fg) * 5) * 256 + p4 * 8;
      const int rs0 = ((0 + q4) ^ ((fg & 1) << 2)) * 32, rs1 = ((4 + q4) ^ ((fg & 1) << 2)) * 32;
#pragma unroll
      for (int n = 0; n < 5; ++n) b[n] = cat8(tr_read(bbase + n * 256 + rs0), tr_read(bbase + n * 256 + rs1));
#pragma unroll
      for (int m = 0; m < 2; ++m)
#pragma unroll
        for (int n = 0; n < 5; ++n) acc[m][n] = MFMA16(a[m], b[n], acc[m][n]);
    }
  }
  RAW_BAR();
  float* RED = (float*)smem;
  constexpr int RR = 84;
#pragma unroll
  for (int m = 0; m < 2; ++m)
#pragma unroll
    for (int n = 0; n < 5; ++n)
#pragma unroll
      for (int i = 0; i < 4; ++i) RED[(kq * 64 + 32 * mi + 16 * m + 4 * fg + i) * RR + 16 * n + fr] = acc[m][n][i];
  __syncthreads();
  for (int it = tid; it < 64 * 9; it += 512) {
    const int row = it / 9, ch = it - row * 9;
    float v[8];
#pragma unroll
    for (int e = 0; e < 8; ++e) v[e] = 0.f;
#pragma unroll
    for (int k = 0; k < 4; ++k) {
      const float4 u0 = *(const float4*)&RED[(k * 64 + row) * RR + ch * 8];
      const float4 u1 = *(const float4*)&RED[(k * 64 + row) * RR + ch * 8 + 4];
      v[0] += u0.x; v[1] += u0.y; v[2] += u0.z; v[3] += u0.w; v[4] += u1.x; v[5] += u1.y; v[6] += u1.z; v[7] += u1.w;
    }
    const u32x4 pk = {pk2(v[0], v[1]), pk2(v[2], v[3]), pk2(v[4], v[5]), pk2(v[6], v[7])};
    *(u32x4*)(out + (size_t)(rb * 64 + row) * D_ + cb * 72 + ch * 8) = pk;
  }
}

extern "C" void kernel_launch(void* const* d_in, const int* in_sizes, int n_in, void* d_out, int out_size,
                              void* d_ws, size_t ws_size, hipStream_t stream) {
  const bf16* x = (const bf16*)d_in[0];
  const bf16* wa = (const bf16*)d_in[1];
  const bf16* woa = (const bf16*)d_in[2];
  const bf16* wb = (const bf16*)d_in[4];
  const bf16* wphi = (const bf16*)d_in[5];
  const bf16* wob = (const bf16*)d_in[6];
  const bf16* gw = (const bf16*)d_in[7];
  char* ws = (char*)d_ws;
  bf16* Q = (bf16*)ws;
  float* KVP = (float*)(ws + 4718592 + 256);
  float* G = (float*)(ws + 4718592 + 256 + 3932160);
  bf16* O = (bf16*)(ws + 4718592 + 256 + 3932160 + 8192);
  k1_qkv<<<256, 512, 0, stream>>>(x, wa, wb, wphi, gw, Q, KVP, G);
  k2_attn<<<256, 512, 0, stream>>>(Q, KVP, G, wphi, O);
  k3_out<<<256, 512, 0, stream>>>(O, woa, wob, (bf16*)d_out);
}
```

```cpp
#include <hip/hip_runtime.h>
#include <hip/hip_bf16.h>
#include <stdint.h>

using bf16 = __hip_bfloat16;
typedef __attribute__((ext_vector_type(8))) short bf16x8;
typedef __attribute__((ext_vector_type(4))) short s16x4;
typedef __attribute__((ext_vector_type(4))) float f32x4;
typedef __attribute__((ext_vector_type(4))) unsigned u32x4;
typedef __attribute__((ext_vector_type(2))) unsigned u32x2;
#define LDS_AS __attribute__((address_space(3)))

static constexpr int S_ = 2048, D_ = 576, N1_ = 1728;

__device__ __forceinline__ unsigned short f2bf(float v) { return __bfloat16_as_ushort(__float2bfloat16(v)); }
__device__ __forceinline__ float bf2f(unsigned short b) { return __uint_as_float(((unsigned)b) << 16); }
__device__ __forceinline__ float bfr(float v) { return bf2f(f2bf(v)); }
__device__ __forceinline__ unsigned pk2(float lo, float hi) { return (unsigned)f2bf(lo) | ((unsigned)f2bf(hi) << 16); }
__device__ __forceinline__ void glds16(const void* g, void* l) {
  unsigned sv;
  const unsigned la = __builtin_amdgcn_readfirstlane((unsigned)(uintptr_t)l);
  asm volatile("s_mov_b32 %0, m0\n\ts_mov_b32 m0, %2\n\ts_nop 0\n\tglobal_load_lds_dwordx4 %1, off\n\ts_mov_b32 m0, %0"
               : "=&s"(sv) : "v"(g), "s"(la) : "memory");
}
__device__ __forceinline__ s16x4 tr_read(const char* p) {
  return __builtin_amdgcn_ds_read_tr16_b64_v4i16((LDS_AS s16x4*)p);
}
__device__ __forceinline__ bf16x8 cat8(s16x4 a, s16x4 b) {
  bf16x8 r; r[0] = a[0]; r[1] = a[1]; r[2] = a[2]; r[3] = a[3]; r[4] = b[0]; r[5] = b[1]; r[6] = b[2]; r[7] = b[3]; return r;
}
#define MFMA16(a, b, c) __builtin_amdgcn_mfma_f32_16x16x32_bf16((a), (b), (c), 0, 0, 0)
#define WAIT_VM(n) asm volatile("s_waitcnt vmcnt(" #n ")" ::: "memory")
#define RAW_BAR() do { asm volatile("s_waitcnt lgkmcnt(0)" ::: "memory"); __builtin_amdgcn_s_barrier(); asm volatile("" ::: "memory"); } while (0)

__device__ __forceinline__ float phiA(float t) {
  float p = t > 0.f ? bfr(t + 1.f) : bfr(__expf(t));
  return bfr(p + 1e-6f);
}

#define WAVE_SYNC() do { __builtin_amdgcn_fence(__ATOMIC_RELEASE, "wavefront"); __builtin_amdgcn_wave_barrier(); \
                         asm volatile("s_waitcnt lgkmcnt(0)" ::: "memory"); __builtin_amdgcn_fence(__ATOMIC_ACQUIRE, "wavefront"); } while (0)

__device__ __forceinline__ void hedgehog_item(unsigned short* cell) {
  const u32x4 r0 = *(const u32x4*)&cell[0], r1 = *(const u32x4*)&cell[8];
  const unsigned r2 = *(const unsigned*)&cell[16];
  float p[18];
#pragma unroll
  for (int j = 0; j < 4; ++j) {
    p[2 * j] = bf2f(r0[j] & 0xffff); p[2 * j + 1] = bf2f(r0[j] >> 16);
    p[8 + 2 * j] = bf2f(r1[j] & 0xffff); p[9 + 2 * j] = bf2f(r1[j] >> 16);
  }
  p[16] = bf2f(r2 & 0xffff); p[17] = bf2f(r2 >> 16);
  float mx = p[0], mn = p[0];
#pragma unroll
  for (int f = 1; f < 18; ++f) { mx = fmaxf(mx, p[f]); mn = fminf(mn, p[f]); }
  float ep[18], en[18], sp = 0.f, sn = 0.f;
#pragma unroll
  for (int f = 0; f < 18; ++f) { ep[f] = __expf(p[f] - mx); sp += ep[f]; en[f] = __expf(mn - p[f]); sn += en[f]; }
  const float ip = 1.f / sp, in_ = 1.f / sn;
  u32x4 w0, w1, w2, w3;
#pragma unroll
  for (int j = 0; j < 4; ++j) {
    w0[j] = pk2(ep[2 * j] * ip, ep[2 * j + 1] * ip); w1[j] = pk2(ep[8 + 2 * j] * ip, ep[9 + 2 * j] * ip);
    w2[j] = pk2(en[2 * j] * in_, en[2 * j + 1] * in_); w3[j] = pk2(en[8 + 2 * j] * in_, en[9 + 2 * j] * in_);
  }
  *(u32x4*)&cell[0] = w0; *(u32x4*)&cell[8] = w1; *(unsigned*)&cell[16] = pk2(ep[16] * ip, ep[17] * ip);
  *(u32x4*)&cell[24] = w2; *(u32x4*)&cell[32] = w3; *(unsigned*)&cell[40] = pk2(en[16] * in_, en[17] * in_);
}

namespace k1 {
constexpr int XS = 16384, WS = 29696, STAGE = XS + WS;
constexpr int WOCT = 3616, WROW = 448;
constexpr int RS = 296;
constexpr int PHS = 200;
constexpr int PH_OFF = 128 * RS * 2;
constexpr int LDS_BYTES = 3 * STAGE;
}
static constexpr int KVH = 960;

__global__ __launch_bounds__(512, 2) void k1_qkv(const bf16* __restrict__ x, const bf16* __restrict__ wA,
                                                 const bf16* __restrict__ wB, const bf16* __restrict__ wphi,
                                                 const bf16* __restrict__ gate_w, bf16* __restrict__ Q,
                                                 float* __restrict__ KVP, float* __restrict__ G) {
  using namespace k1;
  __shared__ __attribute__((aligned(16))) char smem[LDS_BYTES];
  const int tid = threadIdx.x, lane = tid & 63;
  const int wid = __builtin_amdgcn_readfirstlane(tid >> 6);
  const int bid = blockIdx.x, xcd = bid & 7, idx = bid >> 3;
  const int cg = (xcd & 3) * 4 + (idx & 3), rb = (xcd >> 2) * 8 + (idx >> 2);
  const int br = cg >> 3, hg = cg & 7;
  const bf16* W = br ? wB : wA;
  const int fr = lane & 15, fg = lane >> 4;

  const int grow = rb * 128 + cg * 8 + wid;
  u32x4 gx0 = {}, gx1 = {}, gw0 = {}, gw1 = {};
  if (lane < 36) {
    const u32x4* px = (const u32x4*)(x + (size_t)grow * D_ + lane * 16);
    const u32x4* pw = (const u32x4*)(gate_w + lane * 16);
    gx0 = px[0]; gx1 = px[1]; gw0 = pw[0]; gw1 = pw[1];
  }
  const int ph = wid & 3;
  bf16x8 wf[2];
  {
    const unsigned short* wp = (const unsigned short*)wphi + (size_t)(hg * 4 + ph) * 324;
#pragma unroll
    for (int t = 0; t < 2; ++t) {
      const int f = fr + 16 * t;
#pragma unroll
      for (int j = 0; j < 8; ++j) {
        const int d = 8 * fg + j;
        unsigned short v = 0;
        if (br && d < 18 && f < 18) v = wp[d * 18 + f];
        wf[t][j] = (short)v;
      }
    }
  }

  int xoff[2];
#pragma unroll
  for (int i = 0; i < 2; ++i) {
    const int row = 16 * wid + 8 * i + (lane >> 3);
    const int c = (lane & 7) ^ ((row >> 1) & 7);
    xoff[i] = (rb * 128 + row) * D_ + c * 8;
  }
  int woff[4];
#pragma unroll
  for (int m = 0; m < 4; ++m) {
    const int L = 1024 * (wid + 8 * m) + 16 * lane;
    int octet = L / WOCT; const int rem = L - WOCT * octet;
    int r8 = rem / WROW; int ch = (rem - WROW * r8) >> 4;
    if (octet > 7) octet = 7;
    if (r8 > 7) r8 = 7;
    if (ch > 26) ch = 26;
    const int gc = 8 * ch, seg = gc / 72, within = gc - 72 * seg;
    woff[m] = (octet * 8 + r8) * N1_ + seg * D_ + hg * 72 + within;
  }
  auto issue = [&](int t) {
    char* sb = smem + (t % 3) * STAGE;
#pragma unroll
    for (int i = 0; i < 2; ++i) glds16(x + xoff[i] + t * 64, sb + (16 * wid + 8 * i) * 128);
#pragma unroll
    for (int m = 0; m < 4; ++m) {
      const int j = wid + 8 * m;
      if (j < 29) glds16(W + woff[m] + (size_t)t * 64 * N1_, sb + XS + j * 1024);
    }
  };

  const int mi = wid & 1, nj = wid >> 1;
  const int ct0 = nj < 2 ? nj * 4 : 8 + (nj - 2) * 3;
  const bool four = nj < 2;
  const int q4 = fr >> 2, p4 = fr & 3;
  f32x4 acc[4][4];
#pragma unroll
  for (int m = 0; m < 4; ++m)
#pragma unroll
    for (int n = 0; n < 4; ++n) acc[m][n] = f32x4{0.f, 0.f, 0.f, 0.f};

  issue(0);
  issue(1);
#pragma unroll
  for (int t = 0; t < 9; ++t) {
    if (t < 8) { if (wid < 5) WAIT_VM(6); else WAIT_VM(5); }
    else WAIT_VM(0);
    RAW_BAR();
    if (t + 2 < 9) issue(t + 2);
    const char* sb = smem + (t % 3) * STAGE;
    bf16x8 a[2][4], b[2][4];
#pragma unroll
    for (int ks = 0; ks < 2; ++ks) {
#pragma unroll
      for (int m = 0; m < 4; ++m)
        a[ks][m] = *(const bf16x8*)(sb + (64 * mi + 16 * m + fr) * 128 + (((4 * ks + fg) ^ (fr >> 1)) * 16));
      const char* bbase = sb + XS + (4 * ks + fg) * WOCT + q4 * WROW + (16 * ct0 + 4 * p4) * 2;
#pragma unroll
      for (int n = 0; n < 3; ++n) b[ks][n] = cat8(tr_read(bbase + n * 32), tr_read(bbase + n * 32 + 4 * WROW));
      if (four) b[ks][3] = cat8(tr_read(bbase + 3 * 32), tr_read(bbase + 3 * 32 + 4 * WROW));
    }
#pragma unroll
    for (int ks = 0; ks < 2; ++ks) {
#pragma unroll
      for (int m = 0; m < 4; ++m)
#pragma unroll
        for (int n = 0; n < 3; ++n) acc[m][n] = MFMA16(b[ks][n], a[ks][m], acc[m][n]);
      if (four) {
#pragma unroll
        for (int m = 0; m < 4; ++m) acc[m][3] = MFMA16(b[ks][3], a[ks][m], acc[m][3]);
      }
    }
  }

  unsigned short* CT = (unsigned short*)smem;
#pragma unroll
  for (int n = 0; n < 4; ++n) {
    if (n == 3 && !four) continue;
#pragma unroll
    for (int pr = 0; pr < 2; ++pr) {
      const int gc = (ct0 + n) * 16 + 4 * fg + 2 * pr;
      if (gc < 216) {
        const int seg = gc / 72, rem = gc - 72 * seg, h = rem / 18, d = rem - 18 * h;
        const int slot = (seg * 4 + h) * 24 + d;
#pragma unroll
        for (int m = 0; m < 4; ++m) {
          const int row = 64 * mi + 16 * m + fr;
          *(unsigned*)&CT[row * RS + slot] = pk2(acc[m][n][2 * pr], acc[m][n][2 * pr + 1]);
        }
      }
    }
  }
  {
    const int row = tid >> 2, h = tid & 3;
    *(unsigned*)&CT[row * RS + (8 + h) * 24 + 18] = 0x00003F80u;
  }
  __syncthreads();

  const int r64 = (wid >> 2) * 64;
  {
    unsigned* Qd = (unsigned*)Q + ((size_t)(br * 32 + hg * 4 + ph) * S_ + rb * 128 + r64) * 9 + lane;
    int row = r64 + lane / 9, dw = lane % 9;
#pragma unroll
    for (int it = 0; it < 9; ++it) {
      Qd[it * 64] = *(const unsigned*)&CT[row * RS + ph * 24 + 2 * dw];
      row += 7; dw += 1;
      if (dw >= 9) { dw -= 9; row += 1; }
    }
  }
  {
    float s = 0.f;
    const unsigned* a0 = (const unsigned*)&gx0; const unsigned* a1 = (const unsigned*)&gx1;
    const unsigned* b0 = (const unsigned*)&gw0; const unsigned* b1 = (const unsigned*)&gw1;
#pragma unroll
    for (int j = 0; j < 4; ++j) {
      s += bfr(bf2f(a0[j] & 0xffff) * bf2f(b0[j] & 0xffff)) + bfr(bf2f(a0[j] >> 16) * bf2f(b0[j] >> 16));
      s += bfr(bf2f(a1[j] & 0xffff) * bf2f(b1[j] & 0xffff)) + bfr(bf2f(a1[j] >> 16) * bf2f(b1[j] >> 16));
    }
#pragma unroll
    for (int o = 32; o > 0; o >>= 1) s += __shfl_xor(s, o);
    if (lane == 0) G[grow] = 1.f / (1.f + __expf(-bfr(s)));
  }
  unsigned short* PH = (unsigned short*)(smem + PH_OFF);
  if (br) {
#pragma unroll
    for (int j = 0; j < 4; ++j) {
      const int row = r64 + 16 * j + fr;
      u32x4 kb = *(const u32x4*)&CT[row * RS + (4 + ph) * 24 + 8 * fg];
      if (fg == 2) { kb[1] = 0u; kb[2] = 0u; kb[3] = 0u; }
      if (fg == 3) { kb = u32x4{0u, 0u, 0u, 0u}; }
      const bf16x8 kf = __builtin_bit_cast(bf16x8, kb);
      const f32x4 p0 = MFMA16(wf[0], kf, (f32x4{0.f, 0.f, 0.f, 0.f}));
      const f32x4 p1 = MFMA16(wf[1], kf, (f32x4{0.f, 0.f, 0.f, 0.f}));
      unsigned short* dst = PH + row * PHS + ph * 48;
      *(u32x2*)&dst[4 * fg] = u32x2{pk2(p0[0], p0[1]), pk2(p0[2], p0[3])};
      if (fg == 0) *(unsigned*)&dst[16] = pk2(p1[0], p1[1]);
    }
    WAVE_SYNC();
    hedgehog_item(PH + (r64 + lane) * PHS + ph * 48);
  } else {
    unsigned short* cell = CT + (r64 + lane) * RS + (4 + ph) * 24;
    u32x4 r0 = *(const u32x4*)&cell[0], r1 = *(const u32x4*)&cell[8];
    unsigned r2 = *(const unsigned*)&cell[16];
#pragma unroll
    for (int j = 0; j < 4; ++j) {
      r0[j] = pk2(phiA(bf2f(r0[j] & 0xffff)), phiA(bf2f(r0[j] >> 16)));
      r1[j] = pk2(phiA(bf2f(r1[j] & 0xffff)), phiA(bf2f(r1[j] >> 16)));
    }
    r2 = pk2(phiA(bf2f(r2 & 0xffff)), phiA(bf2f(r2 >> 16)));
    *(u32x4*)&cell[0] = r0; *(u32x4*)&cell[8] = r1; *(unsigned*)&cell[16] = r2;
  }
  __syncthreads();

  {
    const int half = wid >> 2;
    const char* abase; int ars;
    if (br) { abase = (const char*)(PH + ph * 48); ars = PHS * 2; }
    else    { abase = (const char*)(CT + (4 + ph) * 24); ars = RS * 2; }
    const char* bbase = (const char*)(CT + (8 + ph) * 24);
    float* dst = KVP + ((size_t)(cg * 16 + rb) * 4 + ph) * KVH;
    const int ncomb = br ? 3 : 2;
#pragma unroll
    for (int c = 0; c < 3; ++c) {
      if (c >= ncomb) break;
      int mt, nt;
      if (br) { const int id = half * 3 + c; mt = id >> 1; nt = id & 1; }
      else    { mt = half; nt = c; }
      f32x4 a3 = {0.f, 0.f, 0.f, 0.f};
#pragma unroll
      for (int ks = 0; ks < 4; ++ks) {
        const int r0 = 32 * ks + 8 * fg + q4;
        const bf16x8 af = cat8(tr_read(abase + r0 * ars + (16 * mt + 4 * p4) * 2),
                               tr_read(abase + (r0 + 4) * ars + (16 * mt + 4 * p4) * 2));
        const bf16x8 bfv = cat8(tr_read(bbase + r0 * (RS * 2) + (16 * nt + 4 * p4) * 2),
                                tr_read(bbase + (r0 + 4) * (RS * 2) + (16 * nt + 4 * p4) * 2));
        a3 = MFMA16(af, bfv, a3);
      }
      const int e = 16 * nt + fr;
      if (e < 19) {
#pragma unroll
        for (int i = 0; i < 4; ++i) {
          const int slot = 16 * mt + 4 * fg + i;
          const bool ok = br ? (slot < 18 || (slot >= 24 && slot < 42)) : (slot < 18);
          if (ok) dst[slot * 20 + e] = a3[i];
        }
      }
    }
  }
}

namespace k2 {
constexpr int KTA_S = 40, KTB_S = 72;
constexpr int KTA_BYTES = 32 * KTA_S * 2;
constexpr int KTB_BYTES = 32 * KTB_S * 2;
constexpr int PHI_OFF = KTA_BYTES + KTB_BYTES;
constexpr int PA = 32, PB = 64;
constexpr int WP_OFF = PHI_OFF + 4 * 64 * PA * 2 + 4 * 64 * PB * 2;
constexpr int PART_OFF = WP_OFF + 1024;
constexpr int LDS_BYTES = PART_OFF + 16 * 5120;
}
typedef unsigned u32x4a4 __attribute__((ext_vector_type(4), aligned(4)));

__global__ __launch_bounds__(512, 2) void k2_attn(const bf16* __restrict__ Q, const float* __restrict__ KVP,
                                                  const float* __restrict__ G, const bf16* __restrict__ wphi,
                                                  bf16* __restrict__ O) {
  using namespace k2;
  __shared__ __attribute__((aligned(16))) char smem[LDS_BYTES];
  unsigned short* KTA = (unsigned short*)smem;
  unsigned short* KTB = (unsigned short*)(smem + KTA_BYTES);
  const int tid = threadIdx.x, lane = tid & 63;
  const int wid = __builtin_amdgcn_readfirstlane(tid >> 6);
  const int fr = lane & 15, fg = lane >> 4;
  const int h = blockIdx.x >> 3, chunk = blockIdx.x & 7;
  const int bw = wid >> 2;
  const int r0 = chunk * 256 + (wid & 3) * 64;
  unsigned short* PHI = (unsigned short*)(smem + PHI_OFF) + (bw ? (4 * 64 * PA + (wid & 3) * 64 * PB) : ((wid & 3) * 64 * PA));

  const unsigned* QdA = (const unsigned*)Q + (size_t)h * S_ * 9;
  const unsigned* QdB = (const unsigned*)Q + (size_t)(32 + h) * S_ * 9;
  u32x4 qa, qb; unsigned qc;
  u32x4 qf[4];
  bf16x8 wf[2];
  {
    const unsigned* src = QdA + (size_t)(r0 + lane) * 9;
    qa = *(const u32x4a4*)src; qb = *(const u32x4a4*)(src + 4); qc = src[8];
  }
#pragma unroll
  for (int rt = 0; rt < 4; ++rt) {
    const unsigned* src = QdB + (size_t)(r0 + 16 * rt + fr) * 9 + 4 * (fg < 2 ? fg : 2);
    qf[rt] = *(const u32x4a4*)src;
  }
  unsigned wpw = 0u;
  if (tid < 162) wpw = ((const unsigned*)(wphi + (size_t)h * 324))[tid];
  float gt[4];
#pragma unroll
  for (int rt = 0; rt < 4; ++rt) gt[rt] = G[r0 + 16 * rt + fr];
  int e4 = 0, bb = 0, slot = 0;
  {
    const int tt = tid < 270 ? tid : 269;
    const int frow = tt / 5; e4 = tt - frow * 5; bb = frow >= 18;
    const int ff = frow - 18 * bb;
    slot = (bb && ff >= 18) ? ff + 6 : ff;
    const int cg = bb * 8 + (h >> 2), hh = h & 3;
    const float* src = KVP + ((size_t)(cg * 16) * 4 + hh) * KVH + slot * 20 + e4 * 4;
    if (wid < 5) {
#pragma unroll
      for (int rb = 0; rb < 16; ++rb) glds16(src + (size_t)rb * 4 * KVH, smem + PART_OFF + rb * 5120 + wid * 1024);
    }
  }
  for (int i = tid; i < PART_OFF / 16; i += 512) ((u32x4*)smem)[i] = u32x4{0u, 0u, 0u, 0u};
  WAIT_VM(0);
  __syncthreads();
  float4 sum = {0.f, 0.f, 0.f, 0.f};
  if (tid < 270) {
#pragma unroll
    for (int rb = 0; rb < 16; ++rb) {
      const f32x4 v = *(const f32x4*)(smem + PART_OFF + rb * 5120 + tid * 16);
      sum.x += v[0]; sum.y += v[1]; sum.z += v[2]; sum.w += v[3];
    }
  }
  unsigned short* WP = (unsigned short*)(smem + WP_OFF);
  if (tid < 162) ((unsigned*)WP)[tid] = wpw;
  if (tid < 270) {
    const float sv[4] = {sum.x, sum.y, sum.z, sum.w};
    unsigned short* KT = bb ? KTB : KTA;
    const int ss = bb ? KTB_S : KTA_S;
#pragma unroll
    for (int i = 0; i < 4; ++i) {
      const int e = e4 * 4 + i;
      const unsigned short v = f2bf(sv[i]);
      if (e < 18) KT[e * ss + slot] = v;
      else if (e == 18) { KT[19 * ss + slot] = v; KT[23 * ss + slot] = v; KT[27 * ss + slot] = v; KT[31 * ss + slot] = v; }
    }
  }
  __syncthreads();
  if (bw == 0) {
    u32x4 w0, w1; unsigned w2;
#pragma unroll
    for (int j = 0; j < 4; ++j) {
      w0[j] = pk2(phiA(bf2f(qa[j] & 0xffff)), phiA(bf2f(qa[j] >> 16)));
      w1[j] = pk2(phiA(bf2f(qb[j] & 0xffff)), phiA(bf2f(qb[j] >> 16)));
    }
    w2 = pk2(phiA(bf2f(qc & 0xffff)), phiA(bf2f(qc >> 16)));
    unsigned short* cell = PHI + lane * PA;
    *(u32x4*)&cell[0] = w0; *(u32x4*)&cell[8] = w1; *(unsigned*)&cell[16] = w2;
  } else {
#pragma unroll
    for (int t = 0; t < 2; ++t) {
      const int f = fr + 16 * t;
#pragma unroll
      for (int j = 0; j < 8; ++j) {
        const int d = 8 * fg + j;
        wf[t][j] = (short)((d < 18 && f < 18) ? WP[d * 18 + f] : (unsigned short)0);
      }
    }
#pragma unroll
    for (int rt = 0; rt < 4; ++rt) {
      u32x4 qv = qf[rt];
      if (fg >= 2) { qv[1] = 0u; qv[2] = 0u; qv[3] = 0u; }
      if (fg == 3) qv[0] = 0u;
      const bf16x8 qq = __builtin_bit_cast(bf16x8, qv);
      const f32x4 p0 = MFMA16(wf[0], qq, (f32x4{0.f, 0.f, 0.f, 0.f}));
      const f32x4 p1 = MFMA16(wf[1], qq, (f32x4{0.f, 0.f, 0.f, 0.f}));
      unsigned short* dst = PHI + (16 * rt + fr) * PB;
      *(u32x2*)&dst[4 * fg] = u32x2{pk2(p0[0], p0[1]), pk2(p0[2], p0[3])};
      if (fg == 0) *(unsigned*)&dst[16] = pk2(p1[0], p1[1]);
    }
    WAVE_SYNC();
    hedgehog_item(PHI + lane * PB);
  }
  WAVE_SYNC();

  if (bw == 0) {
    bf16x8 ka[2];
#pragma unroll
    for (int mt = 0; mt < 2; ++mt) ka[mt] = *(const bf16x8*)&KTA[(16 * mt + fr) * KTA_S + 8 * fg];
#pragma unroll
    for (int rt = 0; rt < 4; ++rt) {
      const bf16x8 pf = *(const bf16x8*)&PHI[(16 * rt + fr) * PA + 8 * fg];
      const f32x4 t0 = MFMA16(ka[0], pf, (f32x4{0.f, 0.f, 0.f, 0.f}));
      const f32x4 t1 = MFMA16(ka[1], pf, (f32x4{0.f, 0.f, 0.f, 0.f}));
      const float sc = gt[rt] / bfr(bfr(t1[3]) + 1e-6f);
      unsigned* orow = (unsigned*)(O + (size_t)(r0 + 16 * rt + fr) * (2 * D_) + h * 18);
      orow[2 * fg] = pk2(bfr(t0[0]) * sc, bfr(t0[1]) * sc);
      orow[2 * fg + 1] = pk2(bfr(t0[2]) * sc, bfr(t0[3]) * sc);
      if (fg == 0) orow[8] = pk2(bfr(t1[0]) * sc, bfr(t1[1]) * sc);
    }
  } else {
    bf16x8 kb0[2], kb1[2];
#pragma unroll
    for (int mt = 0; mt < 2; ++mt) {
      kb0[mt] = *(const bf16x8*)&KTB[(16 * mt + fr) * KTB_S + 8 * fg];
      kb1[mt] = *(const bf16x8*)&KTB[(16 * mt + fr) * KTB_S + 32 + 8 * fg];
    }
#pragma unroll
    for (int rt = 0; rt < 4; ++rt) {
      const bf16x8 ph0 = *(const bf16x8*)&PHI[(16 * rt + fr) * PB + 8 * fg];
      const bf16x8 ph1 = *(const bf16x8*)&PHI[(16 * rt + fr) * PB + 32 + 8 * fg];
      f32x4 t0 = MFMA16(kb0[0], ph0, (f32x4{0.f, 0.f, 0.f, 0.f}));
      t0 = MFMA16(kb1[0], ph1, t0);
      f32x4 t1 = MFMA16(kb0[1], ph0, (f32x4{0.f, 0.f, 0.f, 0.f}));
      t1 = MFMA16(kb1[1], ph1, t1);
      const float sc = (1.f - gt[rt]) / bfr(bfr(t1[3]) + 1e-6f);
      unsigned* orow = (unsigned*)(O + (size_t)(r0 + 16 * rt + fr) * (2 * D_) + D_ + h * 18);
      orow[2 * fg] = pk2(bfr(t0[0]) * sc, bfr(t0[1]) * sc);
      orow[2 * fg + 1] = pk2(bfr(t0[2]) * sc, bfr(t0[3]) * sc);
      if (fg == 0) orow[8] = pk2(bfr(t1[0]) * sc, bfr(t1[1]) * sc);
    }
  }
}

namespace k3 {
constexpr int AS = 16384, BS = 20480, STAGE = AS + BS;
constexpr int NS = 4, KT = 9;
constexpr int LDS_BYTES = NS * STAGE;
}

__global__ __launch_bounds__(512, 2) void k3_out(const bf16* __restrict__ O, const bf16* __restrict__ woA,
                                                 const bf16* __restrict__ woB, bf16* __restrict__ out) {
  using namespace k3;
  __shared__ __attribute__((aligned(16))) char smem[LDS_BYTES];
  const int tid = threadIdx.x, lane = tid & 63;
  const int wid = __builtin_amdgcn_readfirstlane(tid >> 6);
  const int bid = blockIdx.x, xcd = bid & 7, idx = bid >> 3;
  const int rb = xcd * 4 + (idx & 3), cb = idx >> 2;
  const int fr = lane & 15, fg = lane >> 4, q4 = fr >> 2, p4 = fr & 3;

  int aoff[2];
#pragma unroll
  for (int i = 0; i < 2; ++i) {
    const int row = 4 * (2 * wid + i) + fg;
    const int c = fr ^ (row & 15);
    aoff[i] = (rb * 64 + row) * (2 * D_) + c * 8;
  }
  int boff[3];
#pragma unroll
  for (int m = 0; m < 3; ++m) {
    const int j = wid + 8 * m;
    const int st = 4 * j + fg, octet = st / 5, cbk = st % 5;
    const int kio = (fr >> 1) ^ ((octet & 1) << 2);
    int gc = cbk * 16 + (fr & 1) * 8;
    if (gc >= 72) gc = 64;
    boff[m] = (octet * 8 + kio) * D_ + cb * 72 + gc;
  }
  auto issue = [&](int t) {
    char* sb = smem + (t % NS) * STAGE;
#pragma unroll
    for (int i = 0; i < 2; ++i) glds16(O + aoff[i] + t * 128, sb + (2 * wid + i) * 1024);
#pragma unroll
    for (int m = 0; m < 3; ++m) {
      const int j = wid + 8 * m;
      if (j < 20) {
        const int r = boff[m] / D_;
        const int kg = t * 128 + r;
        const bf16* src = (kg < D_) ? (woA + (size_t)t * 128 * D_ + boff[m]) : (woB + ((size_t)kg - D_ - r) * D_ + boff[m]);
        glds16(src, sb + AS + j * 1024);
      }
    }
  };

  const int mi = wid & 1, kq = wid >> 1;
  f32x4 acc[2][5];
#pragma unroll
  for (int m = 0; m < 2; ++m)
#pragma unroll
    for (int n = 0; n < 5; ++n) acc[m][n] = f32x4{0.f, 0.f, 0.f, 0.f};

  issue(0); issue(1); issue(2);
#pragma unroll
  for (int t = 0; t < KT; ++t) {
    if (t < KT - 2) { if (wid < 4) WAIT_VM(10); else WAIT_VM(8); }
    else if (t == KT - 2) { if (wid < 4) WAIT_VM(5); else WAIT_VM(4); }
    else WAIT_VM(0);
    RAW_BAR();
    if (t + 3 < KT) issue(t + 3);
    const char* sb = smem + (t % NS) * STAGE;
    {
      const int ks = kq;
      bf16x8 a[2], b[5];
#pragma unroll
      for (int m = 0; m < 2; ++m) {
        const int row = 32 * mi + 16 * m + fr;
        a[m] = *(const bf16x8*)(sb + row * 256 + (((4 * ks + fg) ^ (row & 15)) * 16));
      }
      const char* bbase = sb + AS + ((4 * ks + fg) * 5) * 256 + p4 * 8;
      const int rs0 = ((0 + q4) ^ ((fg & 1) << 2)) * 32, rs1 = ((4 + q4) ^ ((fg & 1) << 2)) * 32;
#pragma unroll
      for (int n = 0; n < 5; ++n) b[n] = cat8(tr_read(bbase + n * 256 + rs0), tr_read(bbase + n * 256 + rs1));
#pragma unroll
      for (int m = 0; m < 2; ++m)
#pragma unroll
        for (int n = 0; n < 5; ++n) acc[m][n] = MFMA16(a[m], b[n], acc[m][n]);
    }
  }
  RAW_BAR();
  float* RED = (float*)smem;
  constexpr int RR = 84;
#pragma unroll
  for (int m = 0; m < 2; ++m)
#pragma unroll
    for (int n = 0; n < 5; ++n)
#pragma unroll
      for (int i = 0; i < 4; ++i) RED[(kq * 64 + 32 * mi + 16 * m + 4 * fg + i) * RR + 16 * n + fr] = acc[m][n][i];
  __syncthreads();
  for (int it = tid; it < 64 * 9; it += 512) {
    const int row = it / 9, ch = it - row * 9;
    float v[8];
#pragma unroll
    for (int e = 0; e < 8; ++e) v[e] = 0.f;
#pragma unroll
    for (int k = 0; k < 4; ++k) {
      const float4 u0 = *(const float4*)&RED[(k * 64 + row) * RR + ch * 8];
      const float4 u1 = *(const float4*)&RED[(k * 64 + row) * RR + ch * 8 + 4];
      v[0] += u0.x; v[1] += u0.y; v[2] += u0.z; v[3] += u0.w; v[4] += u1.x; v[5] += u1.y; v[6] += u1.z; v[7] += u1.w;
    }
    const u32x4 pk = {pk2(v[0], v[1]), pk2(v[2], v[3]), pk2(v[4], v[5]), pk2(v[6], v[7])};
    *(u32x4*)(out + (size_t)(rb * 64 + row) * D_ + cb * 72 + ch * 8) = pk;
  }
}

extern "C" void kernel_launch(void* const* d_in, const int* in_sizes, int n_in, void* d_out, int out_size,
                              void* d_ws, size_t ws_size, hipStream_t stream) {
  const bf16* x = (const bf16*)d_in[0];
  const bf16* wa = (const bf16*)d_in[1];
  const bf16* woa = (const bf16*)d_in[2];
  const bf16* wb = (const bf16*)d_in[4];
  const bf16* wphi = (const bf16*)d_in[5];
  const bf16* wob = (const bf16*)d_in[6];
  const bf16* gw = (const bf16*)d_in[7];
  char* ws = (char*)d_ws;
  bf16* Q = (bf16*)ws;
  float* KVP = (float*)(ws + 4718592 + 256);
  float* G = (float*)(ws + 4718592 + 256 + 3932160);
  bf16* O = (bf16*)(ws + 4718592 + 256 + 3932160 + 8192);
  k1_qkv<<<256, 512, 0, stream>>>(x, wa, wb, wphi, gw, Q, KVP, G);
  k2_attn<<<256, 512, 0, stream>>>(Q, KVP, G, wphi, O);
  k3_out<<<256, 512, 0, stream>>>(O, woa, wob, (bf16*)d_out);
}
```

```cpp
#include <hip/hip_runtime.h>
#include <hip/hip_bf16.h>
#include <stdint.h>

using bf16 = __hip_bfloat16;
typedef __attribute__((ext_vector_type(8))) short bf16x8;
typedef __attribute__((ext_vector_type(4))) short s16x4;
typedef __attribute__((ext_vector_type(4))) float f32x4;
typedef __attribute__((ext_vector_type(4))) unsigned u32x4;
typedef __attribute__((ext_vector_type(2))) unsigned u32x2;
#define LDS_AS __attribute__((address_space(3)))

static constexpr int S_ = 2048, D_ = 576, N1_ = 1728;

__device__ __forceinline__ unsigned short f2bf(float v) { return __bfloat16_as_ushort(__float2bfloat16(v)); }
__device__ __forceinline__ float bf2f(unsigned short b) { return __uint_as_float(((unsigned)b) << 16); }
__device__ __forceinline__ float bfr(float v) { return bf2f(f2bf(v)); }
__device__ __forceinline__ unsigned pk2(float lo, float hi) { return (unsigned)f2bf(lo) | ((unsigned)f2bf(hi) << 16); }
__device__ __forceinline__ void glds16(const void* g, void* l) {
  unsigned sv;
  const unsigned la = __builtin_amdgcn_readfirstlane((unsigned)(uintptr_t)l);
  asm volatile("s_mov_b32 %0, m0\n\ts_mov_b32 m0, %2\n\ts_nop 0\n\tglobal_load_lds_dwordx4 %1, off\n\ts_mov_b32 m0, %0"
               : "=&s"(sv) : "v"(g), "s"(la) : "memory");
}
__device__ __forceinline__ s16x4 tr_read(const char* p) {
  return __builtin_amdgcn_ds_read_tr16_b64_v4i16((LDS_AS s16x4*)p);
}
__device__ __forceinline__ bf16x8 cat8(s16x4 a, s16x4 b) {
  bf16x8 r; r[0] = a[0]; r[1] = a[1]; r[2] = a[2]; r[3] = a[3]; r[4] = b[0]; r[5] = b[1]; r[6] = b[2]; r[7] = b[3]; return r;
}
#define MFMA16(a, b, c) __builtin_amdgcn_mfma_f32_16x16x32_bf16((a), (b), (c), 0, 0, 0)
#define WAIT_VM(n) asm volatile("s_waitcnt vmcnt(" #n ")" ::: "memory")
#define RAW_BAR() do { asm volatile("s_waitcnt lgkmcnt(0)" ::: "memory"); __builtin_amdgcn_s_barrier(); asm volatile("" ::: "memory"); } while (0)

__device__ __forceinline__ float phiA(float t) {
  float p = t > 0.f ? bfr(t + 1.f) : bfr(__expf(t));
  return bfr(p + 1e-6f);
}

#define WAVE_SYNC() do { __builtin_amdgcn_fence(__ATOMIC_RELEASE, "wavefront"); __builtin_amdgcn_wave_barrier(); \
                         asm volatile("s_waitcnt lgkmcnt(0)" ::: "memory"); __builtin_amdgcn_fence(__ATOMIC_ACQUIRE, "wavefront"); } while (0)

__device__ __forceinline__ void hedgehog_item(unsigned short* cell) {
  const u32x4 r0 = *(const u32x4*)&cell[0], r1 = *(const u32x4*)&cell[8];
  const unsigned r2 = *(const unsigned*)&cell[16];
  float p[18];
#pragma unroll
  for (int j = 0; j < 4; ++j) {
    p[2 * j] = bf2f(r0[j] & 0xffff); p[2 * j + 1] = bf2f(r0[j] >> 16);
    p[8 + 2 * j] = bf2f(r1[j] & 0xffff); p[9 + 2 * j] = bf2f(r1[j] >> 16);
  }
  p[16] = bf2f(r2 & 0xffff); p[17] = bf2f(r2 >> 16);
  float mx = p[0], mn = p[0];
#pragma unroll
  for (int f = 1; f < 18; ++f) { mx = fmaxf(mx, p[f]); mn = fminf(mn, p[f]); }
  float ep[18], en[18], sp = 0.f, sn = 0.f;
#pragma unroll
  for (int f = 0; f < 18; ++f) { ep[f] = __expf(p[f] - mx); sp += ep[f]; en[f] = __expf(mn - p[f]); sn += en[f]; }
  const float ip = 1.f / sp, in_ = 1.f / sn;
  u32x4 w0, w1, w2, w3;
#pragma unroll
  for (int j = 0; j < 4; ++j) {
    w0[j] = pk2(ep[2 * j] * ip, ep[2 * j + 1] * ip); w1[j] = pk2(ep[8 + 2 * j] * ip, ep[9 + 2 * j] * ip);
    w2[j] = pk2(en[2 * j] * in_, en[2 * j + 1] * in_); w3[j] = pk2(en[8 + 2 * j] * in_, en[9 + 2 * j] * in_);
  }
  *(u32x4*)&cell[0] = w0; *(u32x4*)&cell[8] = w1; *(unsigned*)&cell[16] = pk2(ep[16] * ip, ep[17] * ip);
  *(u32x4*)&cell[24] = w2; *(u32x4*)&cell[32] = w3; *(unsigned*)&cell[40] = pk2(en[16] * in_, en[17] * in_);
}

namespace k1 {
constexpr int XS = 16384, WS = 29696, STAGE = XS + WS;
constexpr int WOCT = 3616, WROW = 448;
constexpr int RS = 296;
constexpr int PHS = 200;
constexpr int PH_OFF = 128 * RS * 2;
constexpr int LDS_BYTES = 3 * STAGE;
}
static constexpr int KVH = 960;

__global__ __launch_bounds__(512, 2) void k1_qkv(const bf16* __restrict__ x, const bf16* __restrict__ wA,
                                                 const bf16* __restrict__ wB, const bf16* __restrict__ wphi,
                                                 const bf16* __restrict__ gate_w, bf16* __restrict__ Q,
                                                 float* __restrict__ KVP, float* __restrict__ G) {
  using namespace k1;
  __shared__ __attribute__((aligned(16))) char smem[LDS_BYTES];
  const int tid = threadIdx.x, lane = tid & 63;
  const int wid = __builtin_amdgcn_readfirstlane(tid >> 6);
  const int bid = blockIdx.x, xcd = bid & 7, idx = bid >> 3;
  const int cg = (xcd & 3) * 4 + (idx & 3), rb = (xcd >> 2) * 8 + (idx >> 2);
  const int br = cg >> 3, hg = cg & 7;
  const bf16* W = br ? wB : wA;
  const int fr = lane & 15, fg = lane >> 4;

  const int grow = rb * 128 + cg * 8 + wid;
  u32x4 gx0 = {}, gx1 = {}, gw0 = {}, gw1 = {};
  if (lane < 36) {
    const u32x4* px = (const u32x4*)(x + (size_t)grow * D_ + lane * 16);
    const u32x4* pw = (const u32x4*)(gate_w + lane * 16);
    gx0 = px[0]; gx1 = px[1]; gw0 = pw[0]; gw1 = pw[1];
  }
  const int ph = wid & 3;
  bf16x8 wf[2];
  {
    const unsigned short* wp = (const unsigned short*)wphi + (size_t)(hg * 4 + ph) * 324;
#pragma unroll
    for (int t = 0; t < 2; ++t) {
      const int f = fr + 16 * t;
#pragma unroll
      for (int j = 0; j < 8; ++j) {
        const int d = 8 * fg + j;
        unsigned short v = 0;
        if (br && d < 18 && f < 18) v = wp[d * 18 + f];
        wf[t][j] = (short)v;
      }
    }
  }

  int xoff[2];
#pragma unroll
  for (int i = 0; i < 2; ++i) {
    const int row = 16 * wid + 8 * i + (lane >> 3);
    const int c = (lane & 7) ^ ((row >> 1) & 7);
    xoff[i] = (rb * 128 + row) * D_ + c * 8;
  }
  int woff[4];
#pragma unroll
  for (int m = 0; m < 4; ++m) {
    const int L = 1024 * (wid + 8 * m) + 16 * lane;
    int octet = L / WOCT; const int rem = L - WOCT * octet;
    int r8 = rem / WROW; int ch = (rem - WROW * r8) >> 4;
    if (octet > 7) octet = 7;
    if (r8 > 7) r8 = 7;
    if (ch > 26) ch = 26;
    const int gc = 8 * ch, seg = gc / 72, within = gc - 72 * seg;
    woff[m] = (octet * 8 + r8) * N1_ + seg * D_ + hg * 72 + within;
  }
  auto issue = [&](int t) {
    char* sb = smem + (t % 3) * STAGE;
#pragma unroll
    for (int i = 0; i < 2; ++i) glds16(x + xoff[i] + t * 64, sb + (16 * wid + 8 * i) * 128);
#pragma unroll
    for (int m = 0; m < 4; ++m) {
      const int j = wid + 8 * m;
      if (j < 29) glds16(W + woff[m] + (size_t)t * 64 * N1_, sb + XS + j * 1024);
    }
  };

  const int mi = wid & 1, nj = wid >> 1;
  const int ct0 = nj < 2 ? nj * 4 : 8 + (nj - 2) * 3;
  const bool four = nj < 2;
  const int q4 = fr >> 2, p4 = fr & 3;
  f32x4 acc[4][4];
#pragma unroll
  for (int m = 0; m < 4; ++m)
#pragma unroll
    for (int n = 0; n < 4; ++n) acc[m][n] = f32x4{0.f, 0.f, 0.f, 0.f};

  issue(0);
  issue(1);
  auto compute = [&](int cur) {
    const char* sb = smem + cur * STAGE;
    bf16x8 a[2][4], b[2][4];
#pragma unroll
    for (int ks = 0; ks < 2; ++ks) {
#pragma unroll
      for (int m = 0; m < 4; ++m)
        a[ks][m] = *(const bf16x8*)(sb + (64 * mi + 16 * m + fr) * 128 + (((4 * ks + fg) ^ (fr >> 1)) * 16));
      const char* bbase = sb + XS + (4 * ks + fg) * WOCT + q4 * WROW + (16 * ct0 + 4 * p4) * 2;
#pragma unroll
      for (int n = 0; n < 3; ++n) b[ks][n] = cat8(tr_read(bbase + n * 32), tr_read(bbase + n * 32 + 4 * WROW));
      if (four) b[ks][3] = cat8(tr_read(bbase + 3 * 32), tr_read(bbase + 3 * 32 + 4 * WROW));
    }
#pragma unroll
    for (int ks = 0; ks < 2; ++ks) {
#pragma unroll
      for (int m = 0; m < 4; ++m)
#pragma unroll
        for (int n = 0; n < 3; ++n) acc[m][n] = MFMA16(b[ks][n], a[ks][m], acc[m][n]);
      if (four) {
#pragma unroll
        for (int m = 0; m < 4; ++m) acc[m][3] = MFMA16(b[ks][3], a[ks][m], acc[m][3]);
      }
    }
    };
  int cur = 0;
#pragma unroll 1
  for (int t = 0; t < 8; ++t) {
    if (wid < 5) WAIT_VM(6); else WAIT_VM(5);
    RAW_BAR();
    if (t + 2 < 9) issue(t + 2);
    compute(cur);
    cur = cur == 2 ? 0 : cur + 1;
  }
  WAIT_VM(0);
  RAW_BAR();
  compute(cur);

  unsigned short* CT = (unsigned short*)smem;
#pragma unroll
  for (int n = 0; n < 4; ++n) {
    if (n == 3 && !four) continue;
#pragma unroll
    for (int pr = 0; pr < 2; ++pr) {
      const int gc = (ct0 + n) * 16 + 4 * fg + 2 * pr;
      if (gc < 216) {
        const int seg = gc / 72, rem = gc - 72 * seg, h = rem / 18, d = rem - 18 * h;
        const int slot = (seg * 4 + h) * 24 + d;
#pragma unroll
        for (int m = 0; m < 4; ++m) {
          const int row = 64 * mi + 16 * m + fr;
          *(unsigned*)&CT[row * RS + slot] = pk2(acc[m][n][2 * pr], acc[m][n][2 * pr + 1]);
        }
      }
    }
  }
  {
    const int row = tid >> 2, h = tid & 3;
    *(unsigned*)&CT[row * RS + (8 + h) * 24 + 18] = 0x00003F80u;
  }
  __syncthreads();

  const int r64 = (wid >> 2) * 64;
  {
    unsigned* Qd = (unsigned*)Q + ((size_t)(br * 32 + hg * 4 + ph) * S_ + rb * 128 + r64) * 9 + lane;
    int row = r64 + lane / 9, dw = lane % 9;
#pragma unroll
    for (int it = 0; it < 9; ++it) {
      Qd[it * 64] = *(const unsigned*)&CT[row * RS + ph * 24 + 2 * dw];
      row += 7; dw += 1;
      if (dw >= 9) { dw -= 9; row += 1; }
    }
  }
  {
    float s = 0.f;
    const unsigned* a0 = (const unsigned*)&gx0; const unsigned* a1 = (const unsigned*)&gx1;
    const unsigned* b0 = (const unsigned*)&gw0; const unsigned* b1 = (const unsigned*)&gw1;
#pragma unroll
    for (int j = 0; j < 4; ++j) {
      s += bfr(bf2f(a0[j] & 0xffff) * bf2f(b0[j] & 0xffff)) + bfr(bf2f(a0[j] >> 16) * bf2f(b0[j] >> 16));
      s += bfr(bf2f(a1[j] & 0xffff) * bf2f(b1[j] & 0xffff)) + bfr(bf2f(a1[j] >> 16) * bf2f(b1[j] >> 16));
    }
#pragma unroll
    for (int o = 32; o > 0; o >>= 1) s += __shfl_xor(s, o);
    if (lane == 0) G[grow] = 1.f / (1.f + __expf(-bfr(s)));
  }
  unsigned short* PH = (unsigned short*)(smem + PH_OFF);
  if (br) {
#pragma unroll
    for (int j = 0; j < 4; ++j) {
      const int row = r64 + 16 * j + fr;
      u32x4 kb = *(const u32x4*)&CT[row * RS + (4 + ph) * 24 + 8 * fg];
      if (fg == 2) { kb[1] = 0u; kb[2] = 0u; kb[3] = 0u; }
      if (fg == 3) { kb = u32x4{0u, 0u, 0u, 0u}; }
      const bf16x8 kf = __builtin_bit_cast(bf16x8, kb);
      const f32x4 p0 = MFMA16(wf[0], kf, (f32x4{0.f, 0.f, 0.f, 0.f}));
      const f32x4 p1 = MFMA16(wf[1], kf, (f32x4{0.f, 0.f, 0.f, 0.f}));
      unsigned short* dst = PH + row * PHS + ph * 48;
      *(u32x2*)&dst[4 * fg] = u32x2{pk2(p0[0], p0[1]), pk2(p0[2], p0[3])};
      if (fg == 0) *(unsigned*)&dst[16] = pk2(p1[0], p1[1]);
    }
    WAVE_SYNC();
    hedgehog_item(PH + (r64 + lane) * PHS + ph * 48);
  } else {
    unsigned short* cell = CT + (r64 + lane) * RS + (4 + ph) * 24;
    u32x4 r0 = *(const u32x4*)&cell[0], r1 = *(const u32x4*)&cell[8];
    unsigned r2 = *(const unsigned*)&cell[16];
#pragma unroll
    for (int j = 0; j < 4; ++j) {
      r0[j] = pk2(phiA(bf2f(r0[j] & 0xffff)), phiA(bf2f(r0[j] >> 16)));
      r1[j] = pk2(phiA(bf2f(r1[j] & 0xffff)), phiA(bf2f(r1[j] >> 16)));
    }
    r2 = pk2(phiA(bf2f(r2 & 0xffff)), phiA(bf2f(r2 >> 16)));
    *(u32x4*)&cell[0] = r0; *(u32x4*)&cell[8] = r1; *(unsigned*)&cell[16] = r2;
  }
  __syncthreads();

  {
    const int half = wid >> 2;
    const char* abase; int ars;
    if (br) { abase = (const char*)(PH + ph * 48); ars = PHS * 2; }
    else    { abase = (const char*)(CT + (4 + ph) * 24); ars = RS * 2; }
    const char* bbase = (const char*)(CT + (8 + ph) * 24);
    float* dst = KVP + ((size_t)(cg * 16 + rb) * 4 + ph) * KVH;
    const int ncomb = br ? 3 : 2;
#pragma unroll
    for (int c = 0; c < 3; ++c) {
      if (c >= ncomb) break;
      int mt, nt;
      if (br) { const int id = half * 3 + c; mt = id >> 1; nt = id & 1; }
      else    { mt = half; nt = c; }
      f32x4 a3 = {0.f, 0.f, 0.f, 0.f};
#pragma unroll
      for (int ks = 0; ks < 4; ++ks) {
        const int r0 = 32 * ks + 8 * fg + q4;
        const bf16x8 af = cat8(tr_read(abase + r0 * ars + (16 * mt + 4 * p4) * 2),
                               tr_read(abase + (r0 + 4) * ars + (16 * mt + 4 * p4) * 2));
        const bf16x8 bfv = cat8(tr_read(bbase + r0 * (RS * 2) + (16 * nt + 4 * p4) * 2),
                                tr_read(bbase + (r0 + 4) * (RS * 2) + (16 * nt + 4 * p4) * 2));
        a3 = MFMA16(af, bfv, a3);
      }
      const int e = 16 * nt + fr;
      if (e < 19) {
#pragma unroll
        for (int i = 0; i < 4; ++i) {
          const int slot = 16 * mt + 4 * fg + i;
          const bool ok = br ? (slot < 18 || (slot >= 24 && slot < 42)) : (slot < 18);
          if (ok) dst[slot * 20 + e] = a3[i];
        }
      }
    }
  }
}

namespace k2 {
constexpr int KTA_S = 40, KTB_S = 72;
constexpr int KTA_BYTES = 32 * KTA_S * 2;
constexpr int KTB_BYTES = 32 * KTB_S * 2;
constexpr int PHI_OFF = KTA_BYTES + KTB_BYTES;
constexpr int PA = 32, PB = 64;
constexpr int WP_OFF = PHI_OFF + 4 * 64 * PA * 2 + 4 * 64 * PB * 2;
constexpr int PART_OFF = WP_OFF + 1024;
constexpr int LDS_BYTES = PART_OFF + 16 * 5120;
}
typedef unsigned u32x4a4 __attribute__((ext_vector_type(4), aligned(4)));

__global__ __launch_bounds__(512, 2) void k2_attn(const bf16* __restrict__ Q, const float* __restrict__ KVP,
                                                  const float* __restrict__ G, const bf16* __restrict__ wphi,
                                                  bf16* __restrict__ O) {
  using namespace k2;
  __shared__ __attribute__((aligned(16))) char smem[LDS_BYTES];
  unsigned short* KTA = (unsigned short*)smem;
  unsigned short* KTB = (unsigned short*)(smem + KTA_BYTES);
  const int tid = threadIdx.x, lane = tid & 63;
  const int wid = __builtin_amdgcn_readfirstlane(tid >> 6);
  const int fr = lane & 15, fg = lane >> 4;
  const int h = blockIdx.x >> 3, chunk = blockIdx.x & 7;
  const int bw = wid >> 2;
  const int r0 = chunk * 256 + (wid & 3) * 64;
  unsigned short* PHI = (unsigned short*)(smem + PHI_OFF) + (bw ? (4 * 64 * PA + (wid & 3) * 64 * PB) : ((wid & 3) * 64 * PA));

  const unsigned* QdA = (const unsigned*)Q + (size_t)h * S_ * 9;
  const unsigned* QdB = (const unsigned*)Q + (size_t)(32 + h) * S_ * 9;
  u32x4 qa, qb; unsigned qc;
  u32x4 qf[4];
  bf16x8 wf[2];
  {
    const unsigned* src = QdA + (size_t)(r0 + lane) * 9;
    qa = *(const u32x4a4*)src; qb = *(const u32x4a4*)(src + 4); qc = src[8];
  }
#pragma unroll
  for (int rt = 0; rt < 4; ++rt) {
    const unsigned* src = QdB + (size_t)(r0 + 16 * rt + fr) * 9 + 4 * (fg < 2 ? fg : 2);
    qf[rt] = *(const u32x4a4*)src;
  }
  unsigned wpw = 0u;
  if (tid < 162) wpw = ((const unsigned*)(wphi + (size_t)h * 324))[tid];
  float gt[4];
#pragma unroll
  for (int rt = 0; rt < 4; ++rt) gt[rt] = G[r0 + 16 * rt + fr];
  int e4 = 0, bb = 0, slot = 0;
  {
    const int tt = tid < 270 ? tid : 269;
    const int frow = tt / 5; e4 = tt - frow * 5; bb = frow >= 18;
    const int ff = frow - 18 * bb;
    slot = (bb && ff >= 18) ? ff + 6 : ff;
    const int cg = bb * 8 + (h >> 2), hh = h & 3;
    const float* src = KVP + ((size_t)(cg * 16) * 4 + hh) * KVH + slot * 20 + e4 * 4;
    if (wid < 5) {
#pragma unroll
      for (int rb = 0; rb < 16; ++rb) glds16(src + (size_t)rb * 4 * KVH, smem + PART_OFF + rb * 5120 + wid * 1024);
    }
  }
  for (int i = tid; i < PART_OFF / 16; i += 512) ((u32x4*)smem)[i] = u32x4{0u, 0u, 0u, 0u};
  WAIT_VM(0);
  __syncthreads();
  float4 sum = {0.f, 0.f, 0.f, 0.f};
  if (tid < 270) {
#pragma unroll
    for (int rb = 0; rb < 16; ++rb) {
      const f32x4 v = *(const f32x4*)(smem + PART_OFF + rb * 5120 + tid * 16);
      sum.x += v[0]; sum.y += v[1]; sum.z += v[2]; sum.w += v[3];
    }
  }
  unsigned short* WP = (unsigned short*)(smem + WP_OFF);
  if (tid < 162) ((unsigned*)WP)[tid] = wpw;
  if (tid < 270) {
    const float sv[4] = {sum.x, sum.y, sum.z, sum.w};
    unsigned short* KT = bb ? KTB : KTA;
    const int ss = bb ? KTB_S : KTA_S;
#pragma unroll
    for (int i = 0; i < 4; ++i) {
      const int e = e4 * 4 + i;
      const unsigned short v = f2bf(sv[i]);
      if (e < 18) KT[e * ss + slot] = v;
      else if (e == 18) { KT[19 * ss + slot] = v; KT[23 * ss + slot] = v; KT[27 * ss + slot] = v; KT[31 * ss + slot] = v; }
    }
  }
  __syncthreads();
  if (bw == 0) {
    u32x4 w0, w1; unsigned w2;
#pragma unroll
    for (int j = 0; j < 4; ++j) {
      w0[j] = pk2(phiA(bf2f(qa[j] & 0xffff)), phiA(bf2f(qa[j] >> 16)));
      w1[j] = pk2(phiA(bf2f(qb[j] & 0xffff)), phiA(bf2f(qb[j] >> 16)));
    }
    w2 = pk2(phiA(bf2f(qc & 0xffff)), phiA(bf2f(qc >> 16)));
    unsigned short* cell = PHI + lane * PA;
    *(u32x4*)&cell[0] = w0; *(u32x4*)&cell[8] = w1; *(unsigned*)&cell[16] = w2;
  } else {
#pragma unroll
    for (int t = 0; t < 2; ++t) {
      const int f = fr + 16 * t;
#pragma unroll
      for (int j = 0; j < 8; ++j) {
        const int d = 8 * fg + j;
        wf[t][j] = (short)((d < 18 && f < 18) ? WP[d * 18 + f] : (unsigned short)0);
      }
    }
#pragma unroll
    for (int rt = 0; rt < 4; ++rt) {
      u32x4 qv = qf[rt];
      if (fg >= 2) { qv[1] = 0u; qv[2] = 0u; qv[3] = 0u; }
      if (fg == 3) qv[0] = 0u;
      const bf16x8 qq = __builtin_bit_cast(bf16x8, qv);
      const f32x4 p0 = MFMA16(wf[0], qq, (f32x4{0.f, 0.f, 0.f, 0.f}));
      const f32x4 p1 = MFMA16(wf[1], qq, (f32x4{0.f, 0.f, 0.f, 0.f}));
      unsigned short* dst = PHI + (16 * rt + fr) * PB;
      *(u32x2*)&dst[4 * fg] = u32x2{pk2(p0[0], p0[1]), pk2(p0[2], p0[3])};
      if (fg == 0) *(unsigned*)&dst[16] = pk2(p1[0], p1[1]);
    }
    WAVE_SYNC();
    hedgehog_item(PHI + lane * PB);
  }
  WAVE_SYNC();

  if (bw == 0) {
    bf16x8 ka[2];
#pragma unroll
    for (int mt = 0; mt < 2; ++mt) ka[mt] = *(const bf16x8*)&KTA[(16 * mt + fr) * KTA_S + 8 * fg];
#pragma unroll
    for (int rt = 0; rt < 4; ++rt) {
      const bf16x8 pf = *(const bf16x8*)&PHI[(16 * rt + fr) * PA + 8 * fg];
      const f32x4 t0 = MFMA16(ka[0], pf, (f32x4{0.f, 0.f, 0.f, 0.f}));
      const f32x4 t1 = MFMA16(ka[1], pf, (f32x4{0.f, 0.f, 0.f, 0.f}));
      const float sc = gt[rt] / bfr(bfr(t1[3]) + 1e-6f);
      unsigned* orow = (unsigned*)(O + (size_t)(r0 + 16 * rt + fr) * (2 * D_) + h * 18);
      orow[2 * fg] = pk2(bfr(t0[0]) * sc, bfr(t0[1]) * sc);
      orow[2 * fg + 1] = pk2(bfr(t0[2]) * sc, bfr(t0[3]) * sc);
      if (fg == 0) orow[8] = pk2(bfr(t1[0]) * sc, bfr(t1[1]) * sc);
    }
  } else {
    bf16x8 kb0[2], kb1[2];
#pragma unroll
    for (int mt = 0; mt < 2; ++mt) {
      kb0[mt] = *(const bf16x8*)&KTB[(16 * mt + fr) * KTB_S + 8 * fg];
      kb1[mt] = *(const bf16x8*)&KTB[(16 * mt + fr) * KTB_S + 32 + 8 * fg];
    }
#pragma unroll
    for (int rt = 0; rt < 4; ++rt) {
      const bf16x8 ph0 = *(const bf16x8*)&PHI[(16 * rt + fr) * PB + 8 * fg];
      const bf16x8 ph1 = *(const bf16x8*)&PHI[(16 * rt + fr) * PB + 32 + 8 * fg];
      f32x4 t0 = MFMA16(kb0[0], ph0, (f32x4{0.f, 0.f, 0.f, 0.f}));
      t0 = MFMA16(kb1[0], ph1, t0);
      f32x4 t1 = MFMA16(kb0[1], ph0, (f32x4{0.f, 0.f, 0.f, 0.f}));
      t1 = MFMA16(kb1[1], ph1, t1);
      const float sc = (1.f - gt[rt]) / bfr(bfr(t1[3]) + 1e-6f);
      unsigned* orow = (unsigned*)(O + (size_t)(r0 + 16 * rt + fr) * (2 * D_) + D_ + h * 18);
      orow[2 * fg] = pk2(bfr(t0[0]) * sc, bfr(t0[1]) * sc);
      orow[2 * fg + 1] = pk2(bfr(t0[2]) * sc, bfr(t0[3]) * sc);
      if (fg == 0) orow[8] = pk2(bfr(t1[0]) * sc, bfr(t1[1]) * sc);
    }
  }
}

namespace k3 {
constexpr int AS = 16384, BS = 20480, STAGE = AS + BS;
constexpr int NS = 4, KT = 9;
constexpr int LDS_BYTES = NS * STAGE;
}

__global__ __launch_bounds__(512, 2) void k3_out(const bf16* __restrict__ O, const bf16* __restrict__ woA,
                                                 const bf16* __restrict__ woB, bf16* __restrict__ out) {
  using namespace k3;
  __shared__ __attribute__((aligned(16))) char smem[LDS_BYTES];
  const int tid = threadIdx.x, lane = tid & 63;
  const int wid = __builtin_amdgcn_readfirstlane(tid >> 6);
  const int bid = blockIdx.x, xcd = bid & 7, idx = bid >> 3;
  const int rb = xcd * 4 + (idx & 3), cb = idx >> 2;
  const int fr = lane & 15, fg = lane >> 4, q4 = fr >> 2, p4 = fr & 3;

  int aoff[2];
#pragma unroll
  for (int i = 0; i < 2; ++i) {
    const int row = 4 * (2 * wid + i) + fg;
    const int c = fr ^ (row & 15);
    aoff[i] = (rb * 64 + row) * (2 * D_) + c * 8;
  }
  int boff[3];
#pragma unroll
  for (int m = 0; m < 3; ++m) {
    const int j = wid + 8 * m;
    const int st = 4 * j + fg, octet = st / 5, cbk = st % 5;
    const int kio = (fr >> 1) ^ ((octet & 1) << 2);
    int gc = cbk * 16 + (fr & 1) * 8;
    if (gc >= 72) gc = 64;
    boff[m] = (octet * 8 + kio) * D_ + cb * 72 + gc;
  }
  auto issue = [&](int t) {
    char* sb = smem + (t % NS) * STAGE;
#pragma unroll
    for (int i = 0; i < 2; ++i) glds16(O + aoff[i] + t * 128, sb + (2 * wid + i) * 1024);
#pragma unroll
    for (int m = 0; m < 3; ++m) {
      const int j = wid + 8 * m;
      if (j < 20) {
        const int r = boff[m] / D_;
        const int kg = t * 128 + r;
        const bf16* src = (kg < D_) ? (woA + (size_t)t * 128 * D_ + boff[m]) : (woB + ((size_t)kg - D_ - r) * D_ + boff[m]);
        glds16(src, sb + AS + j * 1024);
      }
    }
  };

  const int mi = wid & 1, kq = wid >> 1;
  f32x4 acc[2][5];
#pragma unroll
  for (int m = 0; m < 2; ++m)
#pragma unroll
    for (int n = 0; n < 5; ++n) acc[m][n] = f32x4{0.f, 0.f, 0.f, 0.f};

  issue(0); issue(1); issue(2);
  auto compute = [&](int cur) {
    const char* sb = smem + cur * STAGE;
    {
      const int ks = kq;
      bf16x8 a[2], b[5];
#pragma unroll
      for (int m = 0; m < 2; ++m) {
        const int row = 32 * mi + 16 * m + fr;
        a[m] = *(const bf16x8*)(sb + row * 256 + (((4 * ks + fg) ^ (row & 15)) * 16));
      }
      const char* bbase = sb + AS + ((4 * ks + fg) * 5) * 256 + p4 * 8;
      const int rs0 = ((0 + q4) ^ ((fg & 1) << 2)) * 32, rs1 = ((4 + q4) ^ ((fg & 1) << 2)) * 32;
#pragma unroll
      for (int n = 0; n < 5; ++n) b[n] = cat8(tr_read(bbase + n * 256 + rs0), tr_read(bbase + n * 256 + rs1));
#pragma unroll
      for (int m = 0; m < 2; ++m)
#pragma unroll
        for (int n = 0; n < 5; ++n) acc[m][n] = MFMA16(a[m], b[n], acc[m][n]);
    }
    };
  int cur = 0;
#pragma unroll 1
  for (int t = 0; t < KT - 2; ++t) {
    if (wid < 4) WAIT_VM(10); else WAIT_VM(8);
    RAW_BAR();
    if (t + 3 < KT) issue(t + 3);
    compute(cur);
    cur = (cur + 1) & 3;
  }
  if (wid < 4) WAIT_VM(5); else WAIT_VM(4);
  RAW_BAR();
  compute(cur);
  cur = (cur + 1) & 3;
  WAIT_VM(0);
  RAW_BAR();
  compute(cur);
  RAW_BAR();
  float* RED = (float*)smem;
  constexpr int RR = 84;
#pragma unroll
  for (int m = 0; m < 2; ++m)
#pragma unroll
    for (int n = 0; n < 5; ++n)
#pragma unroll
      for (int i = 0; i < 4; ++i) RED[(kq * 64 + 32 * mi + 16 * m + 4 * fg + i) * RR + 16 * n + fr] = acc[m][n][i];
  __syncthreads();
  for (int it = tid; it < 64 * 9; it += 512) {
    const int row = it / 9, ch = it - row * 9;
    float v[8];
#pragma unroll
    for (int e = 0; e < 8; ++e) v[e] = 0.f;
#pragma unroll
    for (int k = 0; k < 4; ++k) {
      const float4 u0 = *(const float4*)&RED[(k * 64 + row) * RR + ch * 8];
      const float4 u1 = *(const float4*)&RED[(k * 64 + row) * RR + ch * 8 + 4];
      v[0] += u0.x; v[1] += u0.y; v[2] += u0.z; v[3] += u0.w; v[4] += u1.x; v[5] += u1.y; v[6] += u1.z; v[7] += u1.w;
    }
    const u32x4 pk = {pk2(v[0], v[1]), pk2(v[2], v[3]), pk2(v[4], v[5]), pk2(v[6], v[7])};
    *(u32x4*)(out + (size_t)(rb * 64 + row) * D_ + cb * 72 + ch * 8) = pk;
  }
}

extern "C" void kernel_launch(void* const* d_in, const int* in_sizes, int n_in, void* d_out, int out_size,
                              void* d_ws, size_t ws_size, hipStream_t stream) {
  const bf16* x = (const bf16*)d_in[0];
  const bf16* wa = (const bf16*)d_in[1];
  const bf16* woa = (const bf16*)d_in[2];
  const bf16* wb = (const bf16*)d_in[4];
  const bf16* wphi = (const bf16*)d_in[5];
  const bf16* wob = (const bf16*)d_in[6];
  const bf16* gw = (const bf16*)d_in[7];
  char* ws = (char*)d_ws;
  bf16* Q = (bf16*)ws;
  float* KVP = (float*)(ws + 4718592 + 256);
  float* G = (float*)(ws + 4718592 + 256 + 3932160);
  bf16* O = (bf16*)(ws + 4718592 + 256 + 3932160 + 8192);
  k1_qkv<<<256, 512, 0, stream>>>(x, wa, wb, wphi, gw, Q, KVP, G);
  k2_attn<<<256, 512, 0, stream>>>(Q, KVP, G, wphi, O);
  k3_out<<<256, 512, 0, stream>>>(O, woa, wob, (bf16*)d_out);
}
```
